```python
import torch

def _softmax(x, axis=-1):
    return torch.softmax(x, dim=axis)
def _gelu(x):
    return torch.nn.functional.gelu(x, approximate="tanh")
def _silu(x):
    return torch.nn.functional.silu(x)
def _sigmoid(x):
    return torch.sigmoid(x)
def _layernorm(x, axis=-1, eps=1e-5):
    m = x.mean(dim=axis, keepdim=True)
    v = x.var(dim=axis, keepdim=True, unbiased=False)
    return (x - m) / torch.sqrt(v + eps)
def _scatter_add(tgt, idx, src, axis=0):
    return tgt.clone().index_add_(axis, idx, src)
def _segment_sum(src, seg, n):
    out = torch.zeros((n,) + src.shape[1:], dtype=src.dtype)
    return out.index_add_(0, seg, src)
def _pad(x, pw):
    flat = tuple(v for pair in reversed(pw) for v in pair)
    return torch.nn.functional.pad(x, flat)

kernel_name = 'sequential_preLN__gqa_block_cached__geglu_ffn__decode'

def setup_inputs():

    _w = (lambda a: (lambda t: t.to(torch.bfloat16) if t.is_floating_point() else t)(torch.as_tensor(a)))
    def _A_setup():
        B, Sq, D = 1, 1, 1024
        Hq, Hkv, Sk, Dh = 32, 4, 65536, 128
        assert Hq % Hkv == 0, (Hq, Hkv)
        return {
            "x": _w((lambda *s: torch.randn(*s, dtype=torch.bfloat16) * 0.05)(B, Sq, D)),
            "k_cache": _w((lambda *s: torch.randn(*s, dtype=torch.bfloat16) * 3.2)(B, Hkv, Sk, Dh)),
            "v_cache": _w((lambda *s: torch.randn(*s, dtype=torch.bfloat16) * 0.4)(B, Hkv, Sk, Dh)),
            "wq": _w((lambda *s: torch.randn(*s, dtype=torch.bfloat16) * 3.2)(D, Hq * Dh) * (D ** -0.5)),
            "wo": _w((lambda *s: torch.randn(*s, dtype=torch.bfloat16) * 0.4)(Hq * Dh, D) * ((Hq * Dh) ** -0.5)),
        }
    _A_in = _A_setup()

    def _B_setup():
        B, S, D, F = 1, 1, 1024, 3584
        return {
            "x": _w((lambda *s: torch.randn(*s, dtype=torch.bfloat16) * 0.05)(B, S, D)),
            "w_gate": _w((lambda *s: torch.randn(*s, dtype=torch.bfloat16) * 0.05)(D, F)),
            "w_up": _w((lambda *s: torch.randn(*s, dtype=torch.bfloat16) * 0.05)(D, F)),
            "w_down": _w((lambda *s: torch.randn(*s, dtype=torch.bfloat16) * 0.05)(F, D)),
        }
    _B_in = _B_setup()

    return {
        "A_x": _A_in["x"],
        "A_k_cache": _A_in["k_cache"],
        "A_v_cache": _A_in["v_cache"],
        "A_wq": _A_in["wq"],
        "A_wo": _A_in["wo"],
        "B_x": _B_in["x"],
        "B_w_gate": _B_in["w_gate"],
        "B_w_up": _B_in["w_up"],
        "B_w_down": _B_in["w_down"],
    }

def reference(A_x, A_k_cache, A_v_cache, A_wq, A_wo, B_x, B_w_gate, B_w_up, B_w_down):
    def _A_ref(x, k_cache, v_cache, wq, wo):
        B, Sq, D = x.shape
        Hq, Dh = 32, 128
        Hkv = k_cache.shape[1]
        rep = Hq // Hkv
        q = (lambda x, *d: x.permute(*d))((x @ wq).reshape(B, Sq, Hq, Dh), 0, 2, 1, 3)
        k = (lambda x, n, axis: torch.repeat_interleave(x, n, dim=axis))(k_cache, rep, axis=1)
        v = (lambda x, n, axis: torch.repeat_interleave(x, n, dim=axis))(v_cache, rep, axis=1)
        scores = torch.einsum("bhqd,bhkd->bhqk", q, k) * (Dh ** -0.5)
        p = _softmax(scores, axis=-1)
        o = torch.einsum("bhqk,bhkd->bhqd", p, v)
        return (lambda x, *d: x.permute(*d))(o, 0, 2, 1, 3).reshape(B, Sq, Hq * Dh) @ wo

    def _B_ref(x, w_gate, w_up, w_down):
        return (_gelu(x @ w_gate) * (x @ w_up)) @ w_down

    x = A_x
    h = x + _A_ref(_layernorm(x, axis=-1), A_k_cache, A_v_cache, A_wq, A_wo)
    h2 = h + _B_ref(_layernorm(h, axis=-1), B_w_gate, B_w_up, B_w_down)
    return h2
```

```cpp
#include <hip/hip_runtime.h>
#include <hip/hip_bf16.h>
#include <stdint.h>
#define MIMIC 0
typedef unsigned short u16;
__device__ __forceinline__ float bf2f(u16 b) { return __builtin_bit_cast(float, (unsigned)b << 16); }
__device__ __forceinline__ u16 f2bf(float f) { unsigned u = __builtin_bit_cast(unsigned, f); u += 0x7FFFu + ((u >> 16) & 1u); return (u16)(u >> 16); }
__device__ __forceinline__ float rb(float f) { return bf2f(f2bf(f)); }
__device__ __forceinline__ float mrb(float f) { return MIMIC ? rb(f) : f; }

constexpr int D = 1024, HQ = 32, HKV = 4, SK = 65536, DH = 128, F = 3584, NSPLIT = 64;

__device__ void layernorm_to_lds(const float* xin, float* xn, float* red, int tid, int nthr) {
  float s = 0.f;
  for (int i = tid; i < D; i += nthr) s += xin[i];
  red[tid] = s; __syncthreads();
  for (int o = nthr / 2; o > 0; o >>= 1) { if (tid < o) red[tid] += red[tid + o]; __syncthreads(); }
  float mean = red[0] / D; __syncthreads();
  float v = 0.f;
  for (int i = tid; i < D; i += nthr) { float d = xin[i] - mean; v += d * d; }
  red[tid] = v; __syncthreads();
  for (int o = nthr / 2; o > 0; o >>= 1) { if (tid < o) red[tid] += red[tid + o]; __syncthreads(); }
  float var = red[0] / D; __syncthreads();
  float m = mrb(mean), sd = mrb(sqrtf(mrb(mrb(var) + 1e-5f)));
  for (int i = tid; i < D; i += nthr) xn[i] = mrb(mrb(xin[i] - m) / sd);
  __syncthreads();
}

__global__ void __launch_bounds__(256) k_q(const u16* x, const u16* wq, float* q) {
  __shared__ float xs[D], xn[D], red[256];
  int tid = threadIdx.x;
  for (int i = tid; i < D; i += 256) xs[i] = bf2f(x[i]);
  __syncthreads();
  layernorm_to_lds(xs, xn, red, tid, 256);
  int col = blockIdx.x * 16 + (tid & 15), kg = tid >> 4;
  float s = 0.f;
  for (int k = kg * 64; k < kg * 64 + 64; ++k) s += xn[k] * bf2f(wq[(size_t)k * 4096 + col]);
  red[tid] = s; __syncthreads();
  if (tid < 16) { float t = 0.f; for (int g = 0; g < 16; ++g) t += red[g * 16 + tid]; q[col] = rb(t); }
}

__global__ void __launch_bounds__(256) k_attn(const float* q, const u16* K, const u16* V, float* po, float* pml) {
  __shared__ float qs[8 * DH], sc[8][1024], red[256];
  int tid = threadIdx.x, kvh = blockIdx.x / NSPLIT, sp = blockIdx.x % NSPLIT;
  for (int i = tid; i < 8 * DH; i += 256) qs[i] = q[kvh * 8 * DH + i];
  __syncthreads();
  const u16* Kb = K + ((size_t)kvh * SK + sp * 1024) * DH;
  const u16* Vb = V + ((size_t)kvh * SK + sp * 1024) * DH;
  for (int key = tid; key < 1024; key += 256) {
    float s[8] = {0, 0, 0, 0, 0, 0, 0, 0};
    for (int d = 0; d < DH; d += 8) {
      uint4 kv = *(const uint4*)(Kb + (size_t)key * DH + d);
      unsigned w[4] = {kv.x, kv.y, kv.z, kv.w};
#pragma unroll
      for (int j = 0; j < 4; ++j) {
        float a = bf2f(w[j] & 0xffff), b = bf2f(w[j] >> 16);
#pragma unroll
        for (int h = 0; h < 8; ++h) s[h] += a * qs[h * DH + d + 2 * j] + b * qs[h * DH + d + 2 * j + 1];
      }
    }
#pragma unroll
    for (int h = 0; h < 8; ++h) sc[h][key] = mrb(mrb(s[h]) * 0.08838834764831845f);
  }
  __syncthreads();
  int h = tid >> 5, l = tid & 31;
  float m = -INFINITY;
  for (int k = l; k < 1024; k += 32) m = fmaxf(m, sc[h][k]);
  for (int o = 16; o > 0; o >>= 1) m = fmaxf(m, __shfl_xor(m, o));
  float sum = 0.f;
  for (int k = l; k < 1024; k += 32) { float p = __expf(sc[h][k] - m); sc[h][k] = p; sum += p; }
  for (int o = 16; o > 0; o >>= 1) sum += __shfl_xor(sum, o);
  if (l == 0) { pml[((kvh * 8 + h) * NSPLIT + sp) * 2] = m; pml[((kvh * 8 + h) * NSPLIT + sp) * 2 + 1] = sum; }
  __syncthreads();
  int d = tid & 127, hh = (tid >> 7) * 4;
  float a[4] = {0, 0, 0, 0};
  for (int k = 0; k < 1024; ++k) {
    float v = bf2f(Vb[(size_t)k * DH + d]);
#pragma unroll
    for (int j = 0; j < 4; ++j) a[j] += sc[hh + j][k] * v;
  }
#pragma unroll
  for (int j = 0; j < 4; ++j) po[((size_t)(kvh * 8 + hh + j) * NSPLIT + sp) * DH + d] = a[j];
}

__global__ void __launch_bounds__(128) k_comb(const float* po, const float* pml, float* o) {
  int h = blockIdx.x, d = threadIdx.x;
  float M = -INFINITY;
  for (int s = 0; s < NSPLIT; ++s) M = fmaxf(M, pml[(h * NSPLIT + s) * 2]);
  float L = 0.f, a = 0.f;
  for (int s = 0; s < NSPLIT; ++s) { float w = __expf(pml[(h * NSPLIT + s) * 2] - M); L += w * pml[(h * NSPLIT + s) * 2 + 1]; a += w * po[((size_t)h * NSPLIT + s) * DH + d]; }
  o[h * DH + d] = mrb(a / L);
}

__global__ void __launch_bounds__(256) k_wo(const float* o, const u16* wo, const u16* x, float* h) {
  __shared__ float red[256];
  int tid = threadIdx.x, col = blockIdx.x * 16 + (tid & 15), kg = tid >> 4;
  float s = 0.f;
  for (int k = kg * 256; k < kg * 256 + 256; ++k) s += o[k] * bf2f(wo[(size_t)k * D + col]);
  red[tid] = s; __syncthreads();
  if (tid < 16) { float t = 0.f; for (int g = 0; g < 16; ++g) t += red[g * 16 + tid]; h[col] = mrb(bf2f(x[col]) + mrb(t)); }
}

__global__ void __launch_bounds__(256) k_gu(const float* h, const u16* wg, const u16* wu, float* act) {
  __shared__ float xs[D], xn[D], red[256], red2[256];
  int tid = threadIdx.x;
  for (int i = tid; i < D; i += 256) xs[i] = h[i];
  __syncthreads();
  layernorm_to_lds(xs, xn, red, tid, 256);
  int col = blockIdx.x * 16 + (tid & 15), kg = tid >> 4;
  float s = 0.f, u = 0.f;
  for (int k = kg * 64; k < kg * 64 + 64; ++k) { s += xn[k] * bf2f(wg[(size_t)k * F + col]); u += xn[k] * bf2f(wu[(size_t)k * F + col]); }
  red[tid] = s; red2[tid] = u; __syncthreads();
  if (tid < 16) {
    float g = 0.f, uu = 0.f;
    for (int j = 0; j < 16; ++j) { g += red[j * 16 + tid]; uu += red2[j * 16 + tid]; }
    g = mrb(g); uu = mrb(uu);
    float ge = 0.5f * g * (1.f + tanhf(0.7978845608028654f * (g + 0.044715f * g * g * g)));
    act[col] = mrb(mrb(ge) * uu);
  }
}

__global__ void __launch_bounds__(256) k_down(const float* act, const u16* wd, const float* h, u16* out) {
  __shared__ float red[256];
  int tid = threadIdx.x, col = blockIdx.x * 16 + (tid & 15), kg = tid >> 4;
  float s = 0.f;
  for (int k = kg * 224; k < kg * 224 + 224; ++k) s += act[k] * bf2f(wd[(size_t)k * D + col]);
  red[tid] = s; __syncthreads();
  if (tid < 16) { float t = 0.f; for (int g = 0; g < 16; ++g) t += red[g * 16 + tid]; out[col] = f2bf(h[col] + mrb(t)); }
}

extern "C" void kernel_launch(void* const* d_in, const int* in_sizes, int n_in, void* d_out, int out_size, void* d_ws, size_t ws_size, hipStream_t stream) {
  const u16* x = (const u16*)d_in[0]; const u16* K = (const u16*)d_in[1]; const u16* V = (const u16*)d_in[2];
  const u16* wq = (const u16*)d_in[3]; const u16* wo = (const u16*)d_in[4];
  const u16* wg = (const u16*)d_in[6]; const u16* wu = (const u16*)d_in[7]; const u16* wd = (const u16*)d_in[8];
  float* ws = (float*)d_ws;
  float* q = ws; float* o = ws + 4096; float* h = ws + 8192; float* act = ws + 9216; float* pml = ws + 16384; float* po = ws + 32768;
  k_q<<<256, 256, 0, stream>>>(x, wq, q);
  k_attn<<<256, 256, 0, stream>>>(q, K, V, po, pml);
  k_comb<<<32, 128, 0, stream>>>(po, pml, o);
  k_wo<<<64, 256, 0, stream>>>(o, wo, x, h);
  k_gu<<<224, 256, 0, stream>>>(h, wg, wu, act);
  k_down<<<64, 256, 0, stream>>>(act, wd, h, (u16*)d_out);
}
```

```cpp
#include <hip/hip_runtime.h>
#include <hip/hip_bf16.h>
#include <stdint.h>
#define MIMIC 1
typedef unsigned short u16;
__device__ __forceinline__ float bf2f(u16 b) { return __builtin_bit_cast(float, (unsigned)b << 16); }
__device__ __forceinline__ u16 f2bf(float f) { unsigned u = __builtin_bit_cast(unsigned, f); u += 0x7FFFu + ((u >> 16) & 1u); return (u16)(u >> 16); }
__device__ __forceinline__ float rb(float f) { return bf2f(f2bf(f)); }
__device__ __forceinline__ float mrb(float f) { return MIMIC ? rb(f) : f; }

constexpr int D = 1024, HQ = 32, HKV = 4, SK = 65536, DH = 128, F = 3584, NSPLIT = 64;

__device__ void layernorm_to_lds(const float* xin, float* xn, float* red, int tid, int nthr) {
  float s = 0.f;
  for (int i = tid; i < D; i += nthr) s += xin[i];
  red[tid] = s; __syncthreads();
  for (int o = nthr / 2; o > 0; o >>= 1) { if (tid < o) red[tid] += red[tid + o]; __syncthreads(); }
  float mean = red[0] / D; __syncthreads();
  float v = 0.f;
  for (int i = tid; i < D; i += nthr) { float d = xin[i] - mean; v += d * d; }
  red[tid] = v; __syncthreads();
  for (int o = nthr / 2; o > 0; o >>= 1) { if (tid < o) red[tid] += red[tid + o]; __syncthreads(); }
  float var = red[0] / D; __syncthreads();
  float m = mrb(mean), sd = mrb(sqrtf(mrb(mrb(var) + 1e-5f)));
  for (int i = tid; i < D; i += nthr) xn[i] = mrb(mrb(xin[i] - m) / sd);
  __syncthreads();
}

__global__ void __launch_bounds__(256) k_q(const u16* x, const u16* wq, float* q) {
  __shared__ float xs[D], xn[D], red[256];
  int tid = threadIdx.x;
  for (int i = tid; i < D; i += 256) xs[i] = bf2f(x[i]);
  __syncthreads();
  layernorm_to_lds(xs, xn, red, tid, 256);
  int col = blockIdx.x * 16 + (tid & 15), kg = tid >> 4;
  float s = 0.f;
  for (int k = kg * 64; k < kg * 64 + 64; ++k) s += xn[k] * bf2f(wq[(size_t)k * 4096 + col]);
  red[tid] = s; __syncthreads();
  if (tid < 16) { float t = 0.f; for (int g = 0; g < 16; ++g) t += red[g * 16 + tid]; q[col] = rb(t); }
}

__global__ void __launch_bounds__(256) k_attn(const float* q, const u16* K, const u16* V, float* po, float* pml) {
  __shared__ float qs[8 * DH], sc[8][1024], red[256];
  int tid = threadIdx.x, kvh = blockIdx.x / NSPLIT, sp = blockIdx.x % NSPLIT;
  for (int i = tid; i < 8 * DH; i += 256) qs[i] = q[kvh * 8 * DH + i];
  __syncthreads();
  const u16* Kb = K + ((size_t)kvh * SK + sp * 1024) * DH;
  const u16* Vb = V + ((size_t)kvh * SK + sp * 1024) * DH;
  for (int key = tid; key < 1024; key += 256) {
    float s[8] = {0, 0, 0, 0, 0, 0, 0, 0};
    for (int d = 0; d < DH; d += 8) {
      uint4 kv = *(const uint4*)(Kb + (size_t)key * DH + d);
      unsigned w[4] = {kv.x, kv.y, kv.z, kv.w};
#pragma unroll
      for (int j = 0; j < 4; ++j) {
        float a = bf2f(w[j] & 0xffff), b = bf2f(w[j] >> 16);
#pragma unroll
        for (int h = 0; h < 8; ++h) s[h] += a * qs[h * DH + d + 2 * j] + b * qs[h * DH + d + 2 * j + 1];
      }
    }
#pragma unroll
    for (int h = 0; h < 8; ++h) sc[h][key] = mrb(mrb(s[h]) * 0.08838834764831845f);
  }
  __syncthreads();
  int h = tid >> 5, l = tid & 31;
  float m = -INFINITY;
  for (int k = l; k < 1024; k += 32) m = fmaxf(m, sc[h][k]);
  for (int o = 16; o > 0; o >>= 1) m = fmaxf(m, __shfl_xor(m, o));
  float sum = 0.f;
  for (int k = l; k < 1024; k += 32) { float p = __expf(sc[h][k] - m); sc[h][k] = p; sum += p; }
  for (int o = 16; o > 0; o >>= 1) sum += __shfl_xor(sum, o);
  if (l == 0) { pml[((kvh * 8 + h) * NSPLIT + sp) * 2] = m; pml[((kvh * 8 + h) * NSPLIT + sp) * 2 + 1] = sum; }
  __syncthreads();
  int d = tid & 127, hh = (tid >> 7) * 4;
  float a[4] = {0, 0, 0, 0};
  for (int k = 0; k < 1024; ++k) {
    float v = bf2f(Vb[(size_t)k * DH + d]);
#pragma unroll
    for (int j = 0; j < 4; ++j) a[j] += sc[hh + j][k] * v;
  }
#pragma unroll
  for (int j = 0; j < 4; ++j) po[((size_t)(kvh * 8 + hh + j) * NSPLIT + sp) * DH + d] = a[j];
}

__global__ void __launch_bounds__(128) k_comb(const float* po, const float* pml, float* o) {
  int h = blockIdx.x, d = threadIdx.x;
  float M = -INFINITY;
  for (int s = 0; s < NSPLIT; ++s) M = fmaxf(M, pml[(h * NSPLIT + s) * 2]);
  float L = 0.f, a = 0.f;
  for (int s = 0; s < NSPLIT; ++s) { float w = __expf(pml[(h * NSPLIT + s) * 2] - M); L += w * pml[(h * NSPLIT + s) * 2 + 1]; a += w * po[((size_t)h * NSPLIT + s) * DH + d]; }
  o[h * DH + d] = mrb(a / L);
}

__global__ void __launch_bounds__(256) k_wo(const float* o, const u16* wo, const u16* x, float* h) {
  __shared__ float red[256];
  int tid = threadIdx.x, col = blockIdx.x * 16 + (tid & 15), kg = tid >> 4;
  float s = 0.f;
  for (int k = kg * 256; k < kg * 256 + 256; ++k) s += o[k] * bf2f(wo[(size_t)k * D + col]);
  red[tid] = s; __syncthreads();
  if (tid < 16) { float t = 0.f; for (int g = 0; g < 16; ++g) t += red[g * 16 + tid]; h[col] = mrb(bf2f(x[col]) + mrb(t)); }
}

__global__ void __launch_bounds__(256) k_gu(const float* h, const u16* wg, const u16* wu, float* act) {
  __shared__ float xs[D], xn[D], red[256], red2[256];
  int tid = threadIdx.x;
  for (int i = tid; i < D; i += 256) xs[i] = h[i];
  __syncthreads();
  layernorm_to_lds(xs, xn, red, tid, 256);
  int col = blockIdx.x * 16 + (tid & 15), kg = tid >> 4;
  float s = 0.f, u = 0.f;
  for (int k = kg * 64; k < kg * 64 + 64; ++k) { s += xn[k] * bf2f(wg[(size_t)k * F + col]); u += xn[k] * bf2f(wu[(size_t)k * F + col]); }
  red[tid] = s; red2[tid] = u; __syncthreads();
  if (tid < 16) {
    float g = 0.f, uu = 0.f;
    for (int j = 0; j < 16; ++j) { g += red[j * 16 + tid]; uu += red2[j * 16 + tid]; }
    g = mrb(g); uu = mrb(uu);
    float ge = 0.5f * g * (1.f + tanhf(0.7978845608028654f * (g + 0.044715f * g * g * g)));
    act[col] = mrb(mrb(ge) * uu);
  }
}

__global__ void __launch_bounds__(256) k_down(const float* act, const u16* wd, const float* h, u16* out) {
  __shared__ float red[256];
  int tid = threadIdx.x, col = blockIdx.x * 16 + (tid & 15), kg = tid >> 4;
  float s = 0.f;
  for (int k = kg * 224; k < kg * 224 + 224; ++k) s += act[k] * bf2f(wd[(size_t)k * D + col]);
  red[tid] = s; __syncthreads();
  if (tid < 16) { float t = 0.f; for (int g = 0; g < 16; ++g) t += red[g * 16 + tid]; out[col] = f2bf(h[col] + mrb(t)); }
}

typedef unsigned v4u __attribute__((ext_vector_type(4)));
typedef unsigned long long u64;
template <int NT> __device__ __forceinline__ v4u ldp(const v4u* p) { return NT ? __builtin_nontemporal_load(p) : *p; }
template <int NTK, int NTV>
__global__ void __launch_bounds__(512) k_probe(const v4u* __restrict__ K, const v4u* __restrict__ V, unsigned* sink) {
  constexpr int U = 2, NW = 8;
  const int tid = threadIdx.x, b = blockIdx.x, lane = tid & 63, w = __builtin_amdgcn_readfirstlane(tid >> 6);
  u64 t0 = __builtin_amdgcn_s_memrealtime();
  constexpr int TILE = U * 64, NT_W = 16384 / TILE / NW;
  v4u acc = {0, 0, 0, 0};
  auto tb = [&](int i) -> size_t { return (size_t)b * 16384 + (size_t)(i * NW + w) * TILE + lane; };
  v4u a[U], c[U], a2[U], c2[U];
#pragma unroll
  for (int j = 0; j < U; ++j) { a[j] = ldp<NTK>(K + tb(0) + j * 64); c[j] = ldp<NTV>(V + tb(0) + j * 64); }
  __builtin_amdgcn_sched_barrier(0);
  for (int i = 0; i < NT_W; i += 2) {
#pragma unroll
    for (int j = 0; j < U; ++j) { a2[j] = ldp<NTK>(K + tb(i + 1) + j * 64); c2[j] = ldp<NTV>(V + tb(i + 1) + j * 64); }
    __builtin_amdgcn_sched_barrier(0);
#pragma unroll
    for (int j = 0; j < U; ++j) acc ^= a[j] ^ c[j];
    __builtin_amdgcn_sched_barrier(0);
    if (i + 2 < NT_W) {
#pragma unroll
      for (int j = 0; j < U; ++j) { a[j] = ldp<NTK>(K + tb(i + 2) + j * 64); c[j] = ldp<NTV>(V + tb(i + 2) + j * 64); }
    }
    __builtin_amdgcn_sched_barrier(0);
#pragma unroll
    for (int j = 0; j < U; ++j) acc ^= a2[j] ^ c2[j];
    __builtin_amdgcn_sched_barrier(0);
  }
  unsigned x = acc.x ^ acc.y ^ acc.z ^ acc.w;
  if (x == 0x12345678u) sink[tid] = x;
  u64 t1 = __builtin_amdgcn_s_memrealtime();
  u64 d = t1 - t0; if (d > 20000) d = 20000;
  while (__builtin_amdgcn_s_memrealtime() < t0 + 100 * d) __builtin_amdgcn_s_sleep(8);
}

extern "C" void kernel_launch(void* const* d_in, const int* in_sizes, int n_in, void* d_out, int out_size, void* d_ws, size_t ws_size, hipStream_t stream) {
  const u16* x = (const u16*)d_in[0]; const u16* K = (const u16*)d_in[1]; const u16* V = (const u16*)d_in[2];
  const u16* wq = (const u16*)d_in[3]; const u16* wo = (const u16*)d_in[4];
  const u16* wg = (const u16*)d_in[6]; const u16* wu = (const u16*)d_in[7]; const u16* wd = (const u16*)d_in[8];
  float* ws = (float*)d_ws;
  float* q = ws; float* o = ws + 4096; float* h = ws + 8192; float* act = ws + 9216; float* pml = ws + 16384; float* po = ws + 32768;
  k_probe<1,1><<<256, 512, 0, stream>>>((const v4u*)K, (const v4u*)V, (unsigned*)d_ws + (32<<20));
  k_q<<<256, 256, 0, stream>>>(x, wq, q);
  k_attn<<<256, 256, 0, stream>>>(q, K, V, po, pml);
  k_comb<<<32, 128, 0, stream>>>(po, pml, o);
  k_wo<<<64, 256, 0, stream>>>(o, wo, x, h);
  k_gu<<<224, 256, 0, stream>>>(h, wg, wu, act);
  k_down<<<64, 256, 0, stream>>>(act, wd, h, (u16*)d_out);
}
```

```cpp
#include <hip/hip_runtime.h>
#include <stdint.h>

#define PD 8
#define WD 2
#define TAU 220
#define TOFF 170
#define QDELAY 400
#define QSLEEP 8
#define GEARLY 4
#define DRY 0
#define WMAP 1
#define CODEPF 1
#define ABL 0
#define FULLW 1
#define PREP 1
#define STAG 1
#define QM 0
#define TAU2 200
#define DPOS 1
#define THP 130
#define HSLEEP 0
#define CDELAY 0
#define STAMPS 0

#define DI __device__ __forceinline__
#define LAS __attribute__((address_space(3)))
typedef unsigned v4u __attribute__((ext_vector_type(4)));
typedef unsigned v2u __attribute__((ext_vector_type(2)));
typedef float f32x4 __attribute__((ext_vector_type(4)));
typedef float f32x2 __attribute__((ext_vector_type(2)));
typedef __bf16 bf16x2 __attribute__((ext_vector_type(2)));
typedef __bf16 bf16x8 __attribute__((ext_vector_type(8)));
typedef unsigned long long u64;
typedef __amdgpu_buffer_rsrc_t rsrc_t;

constexpr int NF = 224;
constexpr int NTF = 9;
constexpr float SCALE = 0.08838834764831845f, LOG2E = 1.4426950408889634f, EPS = 1e-5f;
constexpr int AUX_NT = 2, AUX_SC1 = 16;
constexpr int SPIN_CAP = 1 << 18;

constexpr int WS_EPOCH = 0, WS_T0 = 512, WS_HINT = 1024, WS_QP = 4096, WS_ML = WS_QP + 131072, WS_HP = WS_ML + 32768, WS_GU = WS_HP + 131072,
              WS_ST = WS_GU + NF * 1024, WS_DP = WS_ST + NF * 128, WS_OP = WS_DP + 32 * 56 * 128, WS_END = WS_OP + (1 << 20);
constexpr int WS_STAMP = 4 << 20;
constexpr int L_RING = 0, L_Q = 131072, L_ML = L_Q + 2048, L_F = L_ML + 256, L_FLAG = L_F + 128, L_QFLAG = L_FLAG + 16, L_TAG = L_QFLAG + 16, L_RED = L_TAG + 16,
              L_ST = L_RED + 4096, L_SINK = L_ST + 128, L_ACT = L_SINK + 256, L_X = L_ACT + 128, L_END = L_X + 4 * 2304;
constexpr int TSTR = WMAP ? 8192 : 32768;
constexpr int OSTR = 528;

struct Params {
  const void* x; const void* k; const void* v; const void* wq; const void* wo; const void* wg; const void* wu; const void* wd;
  void* out; void* ws;
};

DI rsrc_t mk(const void* p, int bytes) { return __builtin_amdgcn_make_buffer_rsrc((void*)p, 0, bytes, 0x00020000); }
DI float asf(unsigned u) { return __builtin_bit_cast(float, u); }
DI unsigned asu(float f) { return __builtin_bit_cast(unsigned, f); }
DI unsigned cvtpk(float a, float b) { f32x2 v = {a, b}; return __builtin_bit_cast(unsigned, __builtin_convertvector(v, bf16x2)); }
DI float bflo(unsigned u) { return asf(u << 16); }
DI float bfhi(unsigned u) { return asf(u & 0xffff0000u); }
DI float rbf(float a) { return bflo(cvtpk(a, a)); }
DI f32x4 mfma(v4u a, v4u b, f32x4 c) { return __builtin_amdgcn_mfma_f32_16x16x32_bf16(__builtin_bit_cast(bf16x8, a), __builtin_bit_cast(bf16x8, b), c, 0, 0, 0); }
template <int CTRL> DI float dppf(float x) { return asf(__builtin_amdgcn_update_dpp(0u, asu(x), CTRL, 0xf, 0xf, true)); }
constexpr int ROR8 = 0x128, ROR4 = 0x124, XOR1 = 0xB1, XOR2 = 0x4E;
DI float add16(float a, float b) { auto s = __builtin_amdgcn_permlane16_swap(asu(a), asu(b), false, false); return asf(s[0]) + asf(s[1]); }
DI float add32(float a, float b) { auto s = __builtin_amdgcn_permlane32_swap(asu(a), asu(b), false, false); return asf(s[0]) + asf(s[1]); }
DI float max16(float a) { auto s = __builtin_amdgcn_permlane16_swap(asu(a), asu(a), false, false); return fmaxf(asf(s[0]), asf(s[1])); }
DI float max32(float a) { auto s = __builtin_amdgcn_permlane32_swap(asu(a), asu(a), false, false); return fmaxf(asf(s[0]), asf(s[1])); }
DI float wave_sum(float a) { a += dppf<XOR1>(a); a += dppf<XOR2>(a); a += dppf<ROR8>(a); a += dppf<ROR4>(a); a = add16(a, a); return add32(a, a); }
DI float wave_max(float a) { a = fmaxf(a, dppf<XOR1>(a)); a = fmaxf(a, dppf<XOR2>(a)); a = fmaxf(a, dppf<ROR8>(a)); a = fmaxf(a, dppf<ROR4>(a)); return max32(max16(a)); }
DI float rs4(f32x4 v) { return add16(add32(v.x, v.z), add32(v.y, v.w)); }
DI u64 rtc() { return __builtin_amdgcn_s_memrealtime(); }
#define SB() __builtin_amdgcn_sched_barrier(0)
#define CFENCE() asm volatile("" ::: "memory")
template <int N> DI void wait_vm() { asm volatile("s_waitcnt vmcnt(%0)" ::"i"(N) : "memory"); }
DI void wait_lds() { asm volatile("s_waitcnt lgkmcnt(0)" ::: "memory"); }
DI void barrier() { asm volatile("s_waitcnt lgkmcnt(0)\n\ts_barrier" ::: "memory"); SB(); }

struct Tag { unsigned t, b0, b1, b2, b3; };
DI Tag mktag(unsigned t) { return Tag{t, t & 255u, (t >> 8) & 255u, (t >> 16) & 255u, t >> 24}; }
DI v4u enc(f32x4 v, const Tag& g) { return v4u{(asu(v.x) & ~255u) | g.b0, (asu(v.y) & ~255u) | g.b1, (asu(v.z) & ~255u) | g.b2, (asu(v.w) & ~255u) | g.b3}; }
DI f32x4 dec(v4u v, const Tag& g, unsigned& bad) {
  v.x ^= g.b0; v.y ^= g.b1; v.z ^= g.b2; v.w ^= g.b3;
  bad |= (v.x | v.y) | (v.z | v.w);
  return __builtin_bit_cast(f32x4, v);
}
DI bool all_ok(unsigned bad) { return __builtin_amdgcn_ballot_w64((bad & 255u) != 0u) == 0ull; }
DI v4u ld_sc1(rsrc_t r, int vo, int so) { return __builtin_amdgcn_raw_buffer_load_b128(r, vo, so, AUX_SC1); }
DI void st_sc1(v4u v, rsrc_t r, int vo, int so) { __builtin_amdgcn_raw_buffer_store_b128(v, r, vo, so, AUX_SC1); }
DI v4u ld_nt(rsrc_t r, int vo, int so) { return __builtin_amdgcn_raw_buffer_load_b128(r, vo, so, AUX_NT); }
template <int N, int SLEEP = 0> DI void gather(rsrc_t r, int vo, int so, int stride, const Tag& g, f32x4 (&o)[N], int cap) {
#pragma unroll 1
  for (int spin = 0; spin < cap; ++spin) {
    CFENCE();
    v4u v[N];
#pragma unroll
    for (int i = 0; i < N; ++i) v[i] = ld_sc1(r, vo, so + i * stride);
    unsigned bad = 0;
#pragma unroll
    for (int i = 0; i < N; ++i) o[i] = dec(v[i], g, bad);
    if (all_ok(bad)) break;
    if (SLEEP) __builtin_amdgcn_s_sleep(SLEEP);
  }
}

DI v4u tr1(const v4u (&a)[8], int e) {
  const unsigned sel = (e & 1) ? 0x07060302u : 0x05040100u;
  return v4u{__builtin_amdgcn_perm(a[1][e >> 1], a[0][e >> 1], sel), __builtin_amdgcn_perm(a[3][e >> 1], a[2][e >> 1], sel),
             __builtin_amdgcn_perm(a[5][e >> 1], a[4][e >> 1], sel), __builtin_amdgcn_perm(a[7][e >> 1], a[6][e >> 1], sel)};
}
DI void tr8(v4u (&a)[8]) {
  v4u t[8];
#pragma unroll
  for (int e = 0; e < 8; ++e) t[e] = tr1(a, e);
#pragma unroll
  for (int e = 0; e < 8; ++e) { asm volatile("" : "+v"(t[e])); a[e] = t[e]; }
}
DI v4u buildX(float v) {
  const unsigned pk = cvtpk(v, dppf<0x102>(v)), one = 0x3f803f80u;
  return v4u{(unsigned)__builtin_amdgcn_update_dpp(one, pk, 0xE4, 0xf, 0x1, false), (unsigned)__builtin_amdgcn_update_dpp(one, pk, 0x104, 0xf, 0x1, false),
             (unsigned)__builtin_amdgcn_update_dpp(one, pk, 0x108, 0xf, 0x1, false), (unsigned)__builtin_amdgcn_update_dpp(one, pk, 0x10C, 0xf, 0x1, false)};
}
DI f32x4 fold(const f32x4 (&acc)[8], bool s) {
  f32x4 r;
#pragma unroll
  for (int i = 0; i < 4; ++i) r[i] = (s ? acc[4 + i][1] : acc[i][0]) + dppf<ROR8>(s ? acc[i][1] : acc[4 + i][0]);
  return r;
}

#define STAMP(i)
static __device__ const u64 stamp[1] = {0};
#define STAMP_ARG
#define STAMP_PASS

struct Attn {
  rsrc_t rk, rv;
  int base;
  unsigned ring;
  int vk[4], vn;
  LAS unsigned char* L;
  int lane, w;
  unsigned t0;
};
static_assert(PD >= 2 && PD <= 9, "a K tile is read two units after its issue");
template <int J> DI void issueK1(const Attn& A, int t) {
  asm volatile("s_mov_b32 m0, %0\n\ts_nop 0\n\tbuffer_load_dwordx4 %1, %2, %3 offen offset:%4 nt lds"
               ::"s"(A.ring + (t & 3) * 8192 + (J >> 2) * 4096), "v"(A.vk[J & 3]), "s"(A.rk), "s"(A.base + t * TSTR + (J >> 2) * 4096), "i"((J & 3) * 1024) : "memory");
}
template <int OFF> DI void clock_wait(unsigned t0) {
  if (TAU)
    asm volatile("1:\n\ts_memrealtime vcc\n\ts_waitcnt lgkmcnt(0)\n\ts_sub_u32 vcc_lo, vcc_lo, %0\n\ts_cmp_lt_u32 vcc_lo, %1\n\ts_cbranch_scc1 1b"
                 ::"s"(t0), "i"(OFF) : "memory", "scc", "vcc");
}
template <int N> DI void clock_pace(const Attn& A) { clock_wait<TOFF + N * TAU / 16>(A.t0); }
template <int J> struct Ic { static constexpr int v = J; };
template <int U, int J0 = 0, class FI, class FC> DI void unit(const Attn& A, FI issue, FC comp) {
#define STEP(J) if (J >= J0) { if (U >= 0 && (J & 3) == 0) clock_pace<U * 8 + J>(A); wait_vm<PD - 1>(); issue(Ic<J>{}); SB(); comp(Ic<J>{}); SB(); }
  STEP(0) STEP(1) STEP(2) STEP(3) STEP(4) STEP(5) STEP(6) STEP(7)
#undef STEP
}
DI void scoreK(const Attn& A, int t, int u, const v4u (&q)[4], const int (&ra)[4], unsigned (&S)[4], float& mx) {
  f32x4 c = {0.f, 0.f, 0.f, 0.f};
#pragma unroll
  for (int s = 0; s < 4; ++s) c = mfma(*(const LAS v4u*)(A.L + ra[s] + (t & 3) * 8192 + u * 4096), q[s], c);
  mx = fmaxf(fmaxf(mx, c.x), fmaxf(c.y, fmaxf(c.z, c.w)));
  const unsigned a = cvtpk(c.x, c.y), b = cvtpk(c.z, c.w);
  S[2 * u] = cvtpk(bflo(a) * SCALE, bfhi(a) * SCALE);
  S[2 * u + 1] = cvtpk(bflo(b) * SCALE, bfhi(b) * SCALE);
}
DI void expP(unsigned (&S)[4], float m2, float& l) {
#pragma unroll
  for (int i = 0; i < 4; ++i) {
    const float a = __builtin_amdgcn_exp2f(fmaf(bflo(S[i]), LOG2E, -m2)), b = __builtin_amdgcn_exp2f(fmaf(bfhi(S[i]), LOG2E, -m2));
    l += a + b;
    S[i] = cvtpk(a, b);
  }
}
DI void pv(const v4u (&s)[8], const unsigned (&P)[4], f32x4 (&acc)[8], int h) {
  const v4u p = {P[0], P[1], P[2], P[3]};
#pragma unroll
  for (int e = 4 * h; e < 4 * h + 4; ++e) acc[e] = mfma(tr1(s, e), p, acc[e]);
}
DI bool last_of_four(LAS unsigned char* cnt) {
  wait_lds();
  unsigned old = 0;
  if (__builtin_amdgcn_mbcnt_hi(~0u, __builtin_amdgcn_mbcnt_lo(~0u, 0u)) == 0) old = __hip_atomic_fetch_add((LAS unsigned*)cnt, 1u, __ATOMIC_RELAXED, __HIP_MEMORY_SCOPE_WORKGROUP);
  const bool last = __builtin_amdgcn_readfirstlane(old) == 3u;
  CFENCE();
  return last;
}
DI void put_ml(const Attn& A, float m2, float l) {
  l = add32(add16(l, l), add16(l, l));
  if (A.lane < 8) *(LAS f32x2*)(A.L + L_ML + (A.w * 8 + A.lane) * 8) = f32x2{m2, l};
  wait_lds();
  if (A.lane == 0) *(volatile LAS unsigned*)(A.L + L_FLAG + A.w * 4) = 1u;
}
DI void ml_duty(LAS unsigned char* L, rsrc_t rw, int lane, int mlo, unsigned tag) {
#pragma unroll 1
  for (int spin = 0; spin < SPIN_CAP; ++spin) {
    const v4u f = *(volatile LAS v4u*)(L + L_FLAG);
    if (f.x & f.y & f.z & f.w) break;
    __builtin_amdgcn_s_sleep(8);
  }
  CFENCE();
  const f32x2 ml = *(const LAS f32x2*)(L + L_ML + (lane & 31) * 8);
  const float M = max16(fmaxf(ml.x, dppf<ROR8>(ml.x)));
  const float f = __builtin_amdgcn_exp2f(ml.x - M);
  float s = f * ml.y;
  s += dppf<ROR8>(s);
  s = add16(s, s);
  if (lane < 32) *(LAS float*)(L + L_F + lane * 4) = f;
  if (lane < 8) st_sc1(v4u{asu(M), tag, asu(s), tag}, rw, lane * 1024, mlo);
  wait_lds();
}

template <int NT> DI void attn_wave(Attn& A, v4u (&G)[8], rsrc_t rg, int gvo, int gso, unsigned& tag STAMP_ARG) {
  const int g = A.lane >> 4, i = A.lane & 15;
  v4u V0[8], V1[8], V2[8], V3[8];
  unsigned S[NT][4];
  auto none = [](auto) {};
#define IK(t) [&](auto j) { issueK1<decltype(j)::v>(A, t); }
#define IV(t, R) [&](auto j) { constexpr int J = decltype(j)::v; R[J] = ld_nt(A.rv, A.vn + (J & 3) * 1024, A.base + (t) * TSTR + (J >> 2) * 4096); }
#define CO(...) [&](auto j) { constexpr int J = decltype(j)::v; __VA_ARGS__ }
  unit<-1, 4>(A, IK(0), none);
  if (NT > 1) { unit<-1>(A, IK(1), none); unit<-1>(A, IK(2), none); }
  unit<-1>(A, IV(0, V0), none);
  if (NT > 1) {
    unit<-1>(A, IV(1, V1), none);
    asm volatile("s_movk_i32 vcc_hi, 0x7fff\n1:\n\tds_read_b32 %0, %1\n\ts_waitcnt lgkmcnt(0)\n\tv_readfirstlane_b32 vcc_lo, %0\n\ts_sub_u32 vcc_hi, vcc_hi, 1\n\ts_cbranch_scc1 2f\n\t"
                 "s_cmp_eq_u32 vcc_lo, 4\n\ts_cbranch_scc0 1b\n2:" : "=&v"(S[0][0]) : "v"((unsigned)(size_t)A.L + L_QFLAG) : "memory", "scc", "vcc");
    unit<-1>(A, IK(3), none);
  }
  STAMP(1);
  barrier();
  STAMP(2);
  v4u q[4]; int ra[4];
#pragma unroll
  for (int s = 0; s < 4; ++s) {
    q[s] = *(const LAS v4u*)(A.L + L_Q + (i & 7) * 256 + s * 64 + g * 16);
    ra[s] = A.w * 32768 + (4 * (i & 3) + (i >> 2)) * 256 + (((4 * s + g) ^ i) << 4);
  }
  { const v2u tt = *(const LAS v2u*)(A.L + L_TAG); tag = __builtin_amdgcn_readfirstlane(tt.x); A.t0 = __builtin_amdgcn_readfirstlane(tt.y) + (STAG ? A.w * (TAU / 16) : 0); }
  float mx = -INFINITY, l = 0.f, m2;
  f32x4 acc[8];
#pragma unroll
  for (int e = 0; e < 8; ++e) acc[e] = f32x4{0.f, 0.f, 0.f, 0.f};
#define SK(t, u) scoreK(A, t, u, q, ra, S[(t) % NT], mx)
#define EP(t) expP(S[(t) % NT], m2, l)
#define PV(t, R, h) pv(R, S[(t) % NT], acc, h)
  if (NT == 1) {
    wait_vm<8>();
    SK(0, 0); SK(0, 1);
    m2 = rbf(rbf(max32(max16(mx))) * SCALE) * LOG2E;
    EP(0);
    put_ml(A, m2, l);
    PV(0, V0, 0); PV(0, V0, 1);
  } else {
    wait_vm<24>();
    SK(0, 0);
    SB();
    unit<6>(A, IK(4), CO(if (J < 5) SK((J + 1) >> 1, (J + 1) & 1);));
    unit<7>(A, IK(5), CO(if (J < 2) SK(3, J);));
    unit<8>(A, IK(6), CO(if (J < 2) SK(4, J);));
    unit<9>(A, IK(7), CO(if (J < 2) SK(5, J);));
    unit<10>(A, IK(8), CO(if (J < 2) SK(6, J);));
    unit<11>(A, IV(2, V2), CO(if (J < 2) SK(7, J);));
    unit<12>(A, IV(3, V3), CO(if (J < 2) SK(8, J);
                              if (J == 2) { m2 = rbf(rbf(max32(max16(mx))) * SCALE) * LOG2E; EP(0); }
                              if (J == 3 || J == 4) PV(0, V0, J - 3);
                              if (J == 5) EP(1);
                              if (J >= 6) PV(1, V1, J - 6);));
    unit<13>(A, IV(4, V0), CO(if (J == 0) EP(2);
                              if (J == 1 || J == 2) PV(2, V2, J - 1);
                              if (J >= 3) EP(J);));
    unit<14>(A, IV(5, V1), CO(if (J == 0) EP(8);
                              if (J == 1) put_ml(A, m2, l);
                              if (J == 2 || J == 3) PV(3, V3, J - 2);));
    STAMP(3);
    unit<15>(A, IV(6, V2), CO(if (J < 2) PV(4, V0, J);));
    unit<16>(A, IV(7, V3), CO(if (J < 2) PV(5, V1, J);));
    unit<17>(A, IV(8, V0), CO(if (J < 2) PV(6, V2, J);));
    PV(7, V3, 0); PV(7, V3, 1);
    SB();
    if (GEARLY) clock_pace<144>(A);
#pragma unroll
    for (int j = 0; j < GEARLY; ++j) { wait_vm<PD - 1>(); G[j] = ld_nt(rg, gvo, gso + j * 8 * 7168); SB(); }
    PV(8, V0, 0); PV(8, V0, 1);
  }
  STAMP(4);
  if (i < 8) {
#pragma unroll
    for (int e = 0; e < 8; ++e) *(LAS f32x4*)(A.L + A.w * 32768 + i * OSTR + g * 128 + e * 16) = acc[e];
  }
}
DI void publish_o(LAS unsigned char* L, rsrc_t rw, int tid, int grp, int prod, const Tag& T) {
  const int h = tid >> 5, c = tid & 31;
  f32x4 o = {0.f, 0.f, 0.f, 0.f};
#pragma unroll
  for (int w = 0; w < 4; ++w) o += *(const LAS float*)(L + L_F + (w * 8 + h) * 4) * *(const LAS f32x4*)(L + w * 32768 + h * OSTR + c * 16);
  st_sc1(enc(o, T), rw, h * 32768 + c * 16, WS_OP + grp * 8 * 32768 + prod * 512);
}

DI void helper_loads(const Params& p, LAS unsigned char* L, rsrc_t rw, int b, int hw, int lane) {
  const int g = lane >> 4, n = lane & 15, cb = b >> 2, kq = b & 3;
  const rsrc_t rx = mk(p.x, 2048), rq = mk(p.wq, 8 << 20);
  asm volatile("s_mov_b32 m0, %0\n\ts_nop 0\n\tbuffer_load_dwordx4 %1, %2, 0 offen lds\n\tbuffer_load_dwordx4 %1, %2, 0 offen offset:1024 lds\n\t"
               "s_add_u32 m0, m0, 0x800\n\ts_nop 0\n\tbuffer_load_dword off, %3, 0 sc1 lds"
               ::"s"((unsigned)(size_t)L + L_X + hw * 2304), "v"(lane * 16), "s"(rx), "s"(rw) : "memory", "scc");
  const int vo = (4 * (n >> 3) + g) * 8192 + (n & 7) * 16;
#pragma unroll
  for (int j = 0; j < 8; ++j)
    asm volatile("s_mov_b32 m0, %0\n\ts_nop 0\n\tbuffer_load_dwordx4 %1, %2, %3 offen nt lds"
                 ::"s"((unsigned)(size_t)L + hw * 32768 + 24576 + j * 1024), "v"(vo), "s"(rq), "s"((kq * 256 + hw * 64 + j * 8) * 8192 + cb * 128) : "memory");
}
DI void helper_wave(LAS unsigned char* L, rsrc_t rw, int b, int hw, int lane, int grp, u64 t0, unsigned& tag STAMP_ARG) {
  Tag T;
  const int g = lane >> 4, n = lane & 15, cb = b >> 2, kq = b & 3;
  wait_vm<8>();
  const LAS unsigned char* X0 = L + L_X + hw * 2304;
  const v4u xa = *(const LAS v4u*)(X0 + lane * 16), xb = *(const LAS v4u*)(X0 + 1024 + lane * 16);
  const unsigned xo = *(const LAS unsigned short*)(X0 + kq * 512 + hw * 128 + (4 * n + g) * 2), e0 = *(const LAS unsigned*)(X0 + 2048);
  {
    float s = 0.f;
#pragma unroll
    for (int k = 0; k < 4; ++k) s += (bflo(xa[k]) + bfhi(xa[k])) + (bflo(xb[k]) + bfhi(xb[k]));
    const float mean = wave_sum(s) * (1.f / 1024.f);
    float v = 0.f;
#pragma unroll
    for (int k = 0; k < 4; ++k) {
      const float d0 = bflo(xa[k]) - mean, d1 = bfhi(xa[k]) - mean, d2 = bflo(xb[k]) - mean, d3 = bfhi(xb[k]) - mean;
      v += (d0 * d0 + d1 * d1) + (d2 * d2 + d3 * d3);
    }
    const float var = wave_sum(v) * (1.f / 1024.f);
    const float mb = rbf(mean), sd = rbf(sqrtf(rbf(rbf(var) + EPS)));
    const v4u X = buildX(rbf(asf((unsigned)xo << 16) - mb) / sd);
    const unsigned e = __builtin_amdgcn_readfirstlane(e0);
    T = mktag(((e & 0x7f7f7f7fu) + 0x01010101u) ^ (e & 0x80808080u));
    SB();
    wait_vm<0>();
    v4u W[8];
#pragma unroll
    for (int j = 0; j < 8; ++j) W[j] = *(const LAS v4u*)(L + hw * 32768 + 24576 + j * 1024 + lane * 16);
    f32x4 acc[8];
#pragma unroll
    for (int e = 0; e < 8; ++e) acc[e] = mfma(X, tr1(W, e), f32x4{0.f, 0.f, 0.f, 0.f});
    const f32x4 r = fold(acc, n >> 3);
    if (lane < 16) *(LAS f32x4*)(L + L_RED + hw * 256 + ((n & 7) * 8 + (n >> 3) * 4) * 4) = r;
    if (last_of_four(L + L_QFLAG)) {
      const LAS float* rr = (const LAS float*)(L + L_RED + lane * 4);
      __builtin_amdgcn_raw_buffer_store_b64(v2u{asu((rr[0] + rr[64]) + (rr[128] + rr[192])), T.t}, rw, lane * 8, WS_QP + kq * 32768 + cb * 512, AUX_SC1);
      if (TAU && b == 0 && lane == 0) __builtin_amdgcn_raw_buffer_store_b64(v2u{(unsigned)t0, T.t}, rw, 0, WS_T0, AUX_SC1);
    }
  }
  STAMP(1);
  while ((long long)(rtc() - t0) < QDELAY) __builtin_amdgcn_s_sleep(2);
  {
    v4u v[8];
    v2u tz = {0u, T.t};
#pragma unroll 1
    for (int spin = 0; spin < SPIN_CAP; ++spin) {
      CFENCE();
      if (TAU) tz = __builtin_amdgcn_raw_buffer_load_b64(rw, 0, WS_T0, AUX_SC1);
#pragma unroll
      for (int i = 0; i < 8; ++i) v[i] = ld_sc1(rw, lane * 16 + (i & 1) * 1024, WS_QP + (i >> 1) * 32768 + grp * 8192 + hw * 2048);
      unsigned bad = tz.y ^ T.t;
#pragma unroll
      for (int i = 0; i < 8; ++i) bad |= (v[i].y ^ T.t) | (v[i].w ^ T.t);
      if (__builtin_amdgcn_ballot_w64(bad != 0u) == 0ull) break;
      __builtin_amdgcn_s_sleep(QSLEEP);
    }
    if (hw == 0 && lane == 0) *(LAS v2u*)(L + L_TAG) = v2u{T.t, tz.x};
#pragma unroll
    for (int a = 0; a < 2; ++a)
      *(LAS unsigned*)(L + L_Q + hw * 512 + a * 256 + lane * 4) =
          cvtpk((asf(v[a].x) + asf(v[2 + a].x)) + (asf(v[4 + a].x) + asf(v[6 + a].x)), (asf(v[a].z) + asf(v[2 + a].z)) + (asf(v[4 + a].z) + asf(v[6 + a].z)));
  }
  tag = T.t;
  STAMP(2);
  barrier();
}

DI void ffn_tail(const Params& p, LAS unsigned char* L, rsrc_t rw, int cg, int kq, int w, int lane, int grp, int prod, const Tag& T, v4u (&G)[8], unsigned t0 STAMP_ARG) {
  const int g = lane >> 4, n = lane & 15, rec = cg * 4 + kq;
  const int wbytes = (ABL & 2) ? 0 : 7 << 20;
  const rsrc_t rg = mk(p.wg, wbytes), ru = mk(p.wu, wbytes), rd = mk(p.wd, wbytes);
  v4u U[8], Dn[8];
  const int rsel = 4 * (n >> 3) + g, vo = rsel * 7168 + (n & 7) * 16, so = (kq * 256 + w * 64) * 7168 + cg * 128;
  barrier();
  publish_o(L, rw, w * 64 + lane, grp, prod, T);
  barrier();
  const unsigned tp = (unsigned)rtc();
  STAMP(5);
  constexpr int T144 = TOFF + 144 * TAU / 16;
  const unsigned xo = *(const LAS unsigned short*)(L + L_X + kq * 512 + w * 128 + (4 * n + g) * 2);
  if (PREP) {
    if (w == 0) asm volatile("s_mov_b32 m0, %0\n\ts_nop 0\n\tbuffer_load_dword %1, %2, 0 offen sc1 lds"
                             ::"s"((unsigned)(size_t)L + L_SINK), "v"(lane < 32 ? WS_GU + cg * 4096 + lane * 128 : WS_ST + cg * 512 + (lane & 3) * 128), "s"(rw) : "memory");
  }
#pragma unroll
  for (int j = GEARLY; j < 8; ++j) G[j] = ld_nt(rg, vo, so + j * 8 * 7168);
  SB();
  clock_wait<T144 + 8 * TAU2 / 16>(t0);
#pragma unroll
  for (int j = 0; j < 4; ++j) U[j] = ld_nt(ru, vo, so + j * 8 * 7168);
  SB();
  clock_wait<T144 + 12 * TAU2 / 16>(t0);
#pragma unroll
  for (int j = 4; j < 8; ++j) U[j] = ld_nt(ru, vo, so + j * 8 * 7168);
  SB();
  tr8(G);
  SB();
#define LD_D(j) Dn[j] = ld_nt(rd, rsel * 2048 + (n & 7) * 16, (cg * 64 + (j) * 8) * 2048 + kq * 512 + w * 128)
  if (DPOS == 0 || DPOS == 2) {
    clock_wait<T144 + 16 * TAU2 / 16>(t0);
#pragma unroll
    for (int j = 0; j < 4; ++j) LD_D(j);
    SB();
  }
  if (DPOS == 0) {
    clock_wait<T144 + 20 * TAU2 / 16>(t0);
#pragma unroll
    for (int j = 4; j < 8; ++j) LD_D(j);
    SB();
  }
  tr8(U);
  SB();
  if (DPOS == 0) { tr8(Dn); SB(); }
  STAMP(6);
  clock_wait<THP>(tp);
  STAMP(7);
  f32x4 hp[8];
  gather<8, HSLEEP>(rw, g * 4096 + n * 16, WS_HP + kq * 1024 + w * 256, 16384, T, hp, SPIN_CAP);
  STAMP(8);
  if (DPOS) {
#pragma unroll
    for (int j = DPOS == 2 ? 4 : 0; j < 8; ++j) LD_D(j);
    SB();
  }
  const float h = rbf(asf(xo << 16) + rbf(rs4(((hp[0] + hp[1]) + (hp[2] + hp[3])) + ((hp[4] + hp[5]) + (hp[6] + hp[7])))));
  const v4u X = buildX(h);
  const f32x4 z = {0.f, 0.f, 0.f, 0.f};
  const f32x4 gram = mfma(X, X, z);
  f32x4 ag[8], au[8];
#pragma unroll
  for (int e = 0; e < 8; ++e) ag[e] = mfma(X, G[e], z);
#pragma unroll
  for (int e = 0; e < 8; ++e) au[e] = mfma(X, U[e], z);
  if (lane < 8) *(LAS float*)(L + L_ST + lane * 16 + w * 4) = lane == 0 ? gram.x : lane == 1 ? gram.y : gram.x + gram.y;
  const f32x4 rgv = fold(ag, n >> 3), ruv = fold(au, n >> 3);
  if (lane < 32) {
#pragma unroll
    for (int i = 0; i < 4; ++i) *(LAS f32x2*)(L + L_RED + w * 1024 + ((n & 7) * 8 + (n >> 3) * 4 + i) * 16 + g * 8) = f32x2{rgv[i], ruv[i]};
  }
  barrier();
  if (w == 0) {
    const LAS f32x4* r = (const LAS f32x4*)(L + L_RED + lane * 16);
    const LAS f32x4* sp = (const LAS f32x4*)(L + L_ST);
    const f32x4 own = (r[0] + r[64]) + (r[128] + r[192]), sa = sp[0] + sp[1], sb = sp[4];
    st_sc1(enc(own, T), rw, lane * 16, WS_GU + rec * 1024);
    if (lane < 8) st_sc1(v4u{asu((sb.x + sb.y) + (sb.z + sb.w)), T.t, asu((sa.x + sa.y) + (sa.z + sa.w)), T.t}, rw, lane * 16, WS_ST + rec * 128);
    STAMP(9);
    if (CDELAY) __builtin_amdgcn_s_sleep(CDELAY);
    f32x4 gu[4]; v4u st;
#pragma unroll 1
    for (int spin = 0; spin < SPIN_CAP; ++spin) {
      CFENCE();
      v4u v[4];
#pragma unroll
      for (int i = 0; i < 4; ++i) v[i] = ld_sc1(rw, lane * 16 + i * 1024, WS_GU + cg * 4096);
      st = ld_sc1(rw, (lane & 3) * 128, WS_ST + cg * 512);
      unsigned bad = 0;
#pragma unroll
      for (int i = 0; i < 4; ++i) gu[i] = dec(v[i], T, bad);
      if (__builtin_amdgcn_ballot_w64(((bad & 255u) | (st.y ^ T.t) | (st.w ^ T.t)) != 0u) == 0ull) break;
    }
    STAMP(10);
    float s1 = asf(st.x), s2 = asf(st.z);
    s1 += dppf<XOR1>(s1); s2 += dppf<XOR1>(s2); s1 += dppf<XOR2>(s1); s2 += dppf<XOR2>(s2);
    const float mean = s1 * (1.f / 1024.f), var = fmaxf(fmaf(-mean, mean, s2 * (1.f / 1024.f)), 0.f);
    const float mb = rbf(mean), rinv = __builtin_amdgcn_rcpf(rbf(__builtin_amdgcn_sqrtf(rbf(rbf(var) + EPS))));
    const f32x4 t = (gu[0] + gu[1]) + (gu[2] + gu[3]);
    const unsigned gub = cvtpk(fmaf(-mb, t.z, t.x) * rinv, fmaf(-mb, t.w, t.y) * rinv);
    const float gv = bflo(gub), uv = bfhi(gub);
    const float y = gv * fmaf(gv * gv, 0.044715f, 1.f) * (-2.f * 0.7978845608028654f * LOG2E);
    const float a = rbf(gv * __builtin_amdgcn_rcpf(1.f + __builtin_amdgcn_exp2f(y))) * uv;
    *(LAS unsigned short*)(L + L_ACT + (lane & 7) * 16 + (lane >> 3) * 2) = (unsigned short)cvtpk(a, a);
  } else {
    if (rec == 0 && w == 2 && lane == 0) __builtin_amdgcn_raw_buffer_store_b32(T.t, rw, 0, WS_EPOCH, AUX_SC1);
  }
  if (DPOS) { tr8(Dn); SB(); }
  barrier();
  const v4u Xa = *(const LAS v4u*)(L + L_ACT + ((n & 1) * 4 + g) * 16);
  f32x4 ad[8];
#pragma unroll
  for (int e = 0; e < 8; ++e) ad[e] = mfma(Xa, Dn[e], z);
  const f32x4 r = fold(ad, n >> 3);
  if (lane < 16) st_sc1(enc(r, T), rw, ((n & 7) >> 2) * (56 * 128) + (n & 3) * 32 + (n >> 3) * 16, WS_DP + ((kq * 8 + w * 2) * 56 + cg) * 128);
  STAMP(11);
}

DI void wo_tail(const Params& p, LAS unsigned char* L, rsrc_t rw, int j, int w, int lane, const Tag& T0 STAMP_ARG) {
  const int g = lane >> 4, n = lane & 15;
  const rsrc_t ro = mk(p.wo, (ABL & 1) ? 0 : 8 << 20), rx = mk(p.x, 2048), rout = mk(p.out, 2048);
  v4u W[4][8];
#pragma unroll
  for (int t = 0; t < 4; ++t) {
#pragma unroll
    for (int jj = 0; jj < 8; ++jj) {
      wait_vm<WD - 1>();
      W[t][jj] = ld_nt(ro, g * 32 * 2048 + n * 16, (j * 128 + 8 * (jj & 3) + 2 * t + (jj >> 2)) * 2048 + w * 256);
      SB();
    }
    if (t > 0) { tr8(W[t - 1]); SB(); }
  }
  tr8(W[3]);
  SB();
  const unsigned short xo = __builtin_amdgcn_raw_buffer_load_b16(rx, (4 * (lane & 7) + g) * 2, j * 64, 0);
  STAMP(6);
  if (ABL & 4) { while ((long long)(rtc() - stamp[0]) < 2600) __builtin_amdgcn_s_sleep(32); }
  int dry = DRY;
  asm volatile("" : "+s"(dry));
#pragma unroll 1
  for (; dry >= 0; --dry) {
  const int cap = dry ? 1 : SPIN_CAP;
  const Tag T = mktag(dry ? ~T0.t : T0.t);
  v4u ml;
#pragma unroll 1
  for (int spin = 0; spin < cap; ++spin) {
    CFENCE();
    ml = ld_sc1(rw, lane * 16, WS_ML + j * 1024);
    if (__builtin_amdgcn_ballot_w64(((ml.y ^ T.t) | (ml.w ^ T.t)) != 0u) == 0ull) break;
    __builtin_amdgcn_s_sleep(16);
  }
  const float wp = __builtin_amdgcn_exp2f(asf(ml.x) - wave_max(asf(ml.x)));
  const float cp = wp / wave_sum(wp * asf(ml.z));
  float c[4];
#pragma unroll
  for (int i = 0; i < 4; ++i) {
    const float a = asf(__builtin_amdgcn_readlane(asu(cp), 8 * w + 2 * i)), bb = asf(__builtin_amdgcn_readlane(asu(cp), 8 * w + 2 * i + 1));
    c[i] = lane < 32 ? a : bb;
  }
  STAMP(7);
  f32x4 op[4];
  gather<4>(rw, lane * 16, WS_OP + j * 32768 + w * 4096, 1024, T, op, cap);
  const f32x4 o4 = (c[0] * op[0] + c[1] * op[1]) + (c[2] * op[2] + c[3] * op[3]);
  *(LAS f32x2*)(L + L_RED + w * 512 + (lane & 31) * 16 + (lane >> 5) * 8) = f32x2{add32(o4.x, o4.z), add32(o4.y, o4.w)};
  barrier();
  STAMP(8);
  const LAS f32x2* r = (const LAS f32x2*)(L + L_RED + lane * 8);
  const f32x2 o2 = ((r[0] + r[64]) + (r[128] + r[192])) + ((r[256] + r[320]) + (r[384] + r[448]));
  const unsigned ob = cvtpk(o2.x, o2.y);
  f32x4 acc[8];
#pragma unroll
  for (int e = 0; e < 8; ++e) acc[e] = f32x4{0.f, 0.f, 0.f, 0.f};
#define BC(k) (unsigned)__builtin_amdgcn_update_dpp(0u, ob, 0x150 + (k), 0xf, 0xf, true)
  const v4u X[4] = {v4u{BC(0), BC(1), BC(2), BC(3)}, v4u{BC(4), BC(5), BC(6), BC(7)}, v4u{BC(8), BC(9), BC(10), BC(11)}, v4u{BC(12), BC(13), BC(14), BC(15)}};
#undef BC
#pragma unroll
  for (int t = 0; t < 4; ++t)
#pragma unroll
    for (int e = 0; e < 8; ++e) acc[e] = mfma(X[t], W[t][e], acc[e]);
  const bool hi = g & 1;
  const f32x4 y = {hi ? acc[4].x : acc[0].x, hi ? acc[5].x : acc[1].x, hi ? acc[6].x : acc[2].x, hi ? acc[7].x : acc[3].x};
  if (lane < 32) st_sc1(enc(y, T), rw, n * 32 + g * 16, WS_HP + j * 4096 + w * 512);
  STAMP(9);
  if (w == 0) {
    f32x4 hp[4];
    gather<4>(rw, (lane >> 3) * 4096 + (lane & 7) * 16, WS_HP + j * 128, 32768, T, hp, cap);
    float hs = rs4((hp[0] + hp[1]) + (hp[2] + hp[3]));
    hs += dppf<ROR8>(hs);
    const float h = rbf(asf((unsigned)xo << 16) + rbf(hs));
    STAMP(10);
    f32x4 dp[7];
    gather<7>(rw, lane * 16, WS_DP + j * 56 * 128, 1024, T, dp, cap);
    float ds = rs4(((dp[0] + dp[1]) + (dp[2] + dp[3])) + ((dp[4] + dp[5]) + dp[6]));
    ds += dppf<ROR8>(ds);
    const float o = h + rbf(ds);
    if ((lane & 8) == 0) __builtin_amdgcn_raw_buffer_store_b16((unsigned short)cvtpk(o, o), rout, (4 * (lane & 7) + g) * 2, j * 64, 0);
    STAMP(11);
  }
  if (dry) { wait_vm<0>(); barrier(); }
  }
}

__global__ void __launch_bounds__(512, 2) decode_block_kernel(Params p) {
  __shared__ __attribute__((aligned(16))) unsigned char smem[L_END];
  LAS unsigned char* L = (LAS unsigned char*)smem;
  const int tid = threadIdx.x, lane = tid & 63, w = __builtin_amdgcn_readfirstlane(tid >> 6), b = blockIdx.x;
  STAMP(0);
  const rsrc_t rw = mk(p.ws, WS_END);
  const int w4 = w & 3;
  int r0 = w >> 2, r1 = r0, r2 = r0, r3 = r0;
  asm volatile("" : "+s"(r1));
  asm volatile("" : "+s"(r2));
  asm volatile("" : "+s"(r3));
  const u64 t0 = rtc();
  if (r0 == 0) {
    u64 pc; int len;
    asm volatile("s_getpc_b64 %0\n.Lcode_from:\n\ts_mov_b32 %1, .Lcode_to-.Lcode_from" : "=s"(pc), "=s"(len));
    asm volatile("s_mov_b32 m0, %0\n\ts_nop 0\n\tbuffer_load_dword %1, %2, %3 offen lds"
                 ::"s"((unsigned)(size_t)L + L_SINK), "v"((lane & 3) * 128), "s"(mk((const void*)pc, len)), "s"(((b >> 3) * 4 + w) * 384) : "memory");
    helper_loads(p, L, rw, b, w4, lane);
  }
  if (tid < 8) *(LAS unsigned*)(L + L_FLAG + lane * 4) = 0u;
  const int isW = (NF - 1 - b) >> 31, gF = (b * 1171) >> 16, jW = b - NF;
  const int grp = (gF & ~isW) | ((jW >> 3) & isW), prod = ((b - gF * 56) & ~isW) | ((56 + (jW & 7)) & isW);
  Attn A;
  A.rk = mk(p.k, 64 << 20); A.rv = mk(p.v, 64 << 20);
  A.base = grp * (16 << 20) + (((prod * 4 * NTF + w4 * (WMAP ? NTF : 1)) & ~isW) | ((56 * 4 * NTF + (prod - 56) * 4 + w4) & isW)) * 8192;
  A.ring = (unsigned)(size_t)L + w4 * 32768;
#pragma unroll
  for (int j = 0; j < 4; ++j) A.vk[j] = (lane >> 4) * 256 + (((lane & 15) ^ (4 * (lane >> 4) + j)) << 4);
  A.vn = lane * 16; A.L = L; A.lane = lane; A.w = w4;
  SB();
  barrier();
  if (r1 != 0) { issueK1<0>(A, 0); issueK1<1>(A, 0); issueK1<2>(A, 0); issueK1<3>(A, 0); }
  STAMP(11);
  unsigned tag;
  if (r2 == 0) helper_wave(L, rw, b, w4, lane, grp, t0, tag STAMP_PASS);
  v4u G[8];
  int b0 = b;
  asm volatile("" : "+s"(b0));
  if (b0 < NF) {
    const int cg = QM ? (b & 7) * 7 + (b >> 5) : b >> 2, kq = QM ? (b >> 3) & 3 : b & 3;
    const int gvo = (4 * ((lane & 15) >> 3) + (lane >> 4)) * 7168 + (lane & 7) * 16, gso = (kq * 256 + w4 * 64) * 7168 + cg * 128;
    if (r3 != 0) {
      attn_wave<NTF>(A, G, mk(p.wg, (ABL & 2) ? 0 : 7 << 20), gvo, gso, tag STAMP_PASS);
      ffn_tail(p, L, rw, cg, kq, w4, lane, grp, prod, mktag(tag), G, A.t0 STAMP_PASS);
    }
    if (w == 0) ml_duty(L, rw, lane, WS_ML + grp * 8192 + prod * 16, tag);
  }
  if (b >= NF) {
    if (r3 != 0) attn_wave<1>(A, G, rw, 0, 0, tag STAMP_PASS);
    if (w == 0) ml_duty(L, rw, lane, WS_ML + grp * 8192 + prod * 16, tag);
    barrier();
    const Tag T = mktag(tag);
    if (w >= 4) publish_o(L, rw, tid - 256, grp, prod, T);
    STAMP(5);
    wo_tail(p, L, rw, jW, w, lane, T STAMP_PASS);
    asm volatile(".Lcode_to:");
  }
}

extern "C" void kernel_launch(void* const* d_in, const int* in_sizes, int n_in, void* d_out, int out_size, void* d_ws, size_t ws_size, hipStream_t stream) {
  Params p;
  p.x = d_in[0]; p.k = d_in[1]; p.v = d_in[2]; p.wq = d_in[3]; p.wo = d_in[4]; p.wg = d_in[6]; p.wu = d_in[7]; p.wd = d_in[8];
  p.out = d_out; p.ws = d_ws;
  decode_block_kernel<<<256, 512, 0, stream>>>(p);
}
```

```cpp
#define STAMPS 1
#define AMP 0
#define AMPW 0
#define AMPS 0
#include <hip/hip_runtime.h>
#include <stdint.h>

#define PD 8
#define WD 2
#define TAU 220
#define TOFF 170
#define QDELAY 400
#define QSLEEP 8
#define GEARLY 4
#define DRY 0
#define WMAP 1
#define CODEPF 1
#define ABL 0
#define FULLW 1
#define PREP 1
#define STAG 1
#define QM 0
#define TAU2 200
#define DPOS 1
#define THP 130
#define HSLEEP 0
#define CDELAY 0

#define DI __device__ __forceinline__
#define LAS __attribute__((address_space(3)))
typedef unsigned v4u __attribute__((ext_vector_type(4)));
typedef unsigned v2u __attribute__((ext_vector_type(2)));
typedef float f32x4 __attribute__((ext_vector_type(4)));
typedef float f32x2 __attribute__((ext_vector_type(2)));
typedef __bf16 bf16x2 __attribute__((ext_vector_type(2)));
typedef __bf16 bf16x8 __attribute__((ext_vector_type(8)));
typedef unsigned long long u64;
typedef __amdgpu_buffer_rsrc_t rsrc_t;

constexpr int NF = 224;
constexpr int NTF = 9;
constexpr float SCALE = 0.08838834764831845f, LOG2E = 1.4426950408889634f, EPS = 1e-5f;
constexpr int AUX_NT = 2, AUX_SC1 = 16;
constexpr int SPIN_CAP = 1 << 18;

constexpr int WS_EPOCH = 0, WS_T0 = 512, WS_HINT = 1024, WS_QP = 4096, WS_ML = WS_QP + 131072, WS_HP = WS_ML + 32768, WS_GU = WS_HP + 131072,
              WS_ST = WS_GU + NF * 1024, WS_DP = WS_ST + NF * 128, WS_OP = WS_DP + 32 * 56 * 128, WS_END = WS_OP + (1 << 20);
constexpr int WS_STAMP = 4 << 20;
constexpr int L_RING = 0, L_Q = 131072, L_ML = L_Q + 2048, L_F = L_ML + 256, L_FLAG = L_F + 128, L_QFLAG = L_FLAG + 16, L_TAG = L_QFLAG + 16, L_RED = L_TAG + 16,
              L_ST = L_RED + 4096, L_SINK = L_ST + 128, L_ACT = L_SINK + 256, L_X = L_ACT + 128, L_END = L_X + 4 * 2304;
constexpr int TSTR = WMAP ? 8192 : 32768;
constexpr int OSTR = 528;

struct Params {
  const void* x; const void* k; const void* v; const void* wq; const void* wo; const void* wg; const void* wu; const void* wd;
  void* out; void* ws;
};

DI rsrc_t mk(const void* p, int bytes) { return __builtin_amdgcn_make_buffer_rsrc((void*)p, 0, bytes, 0x00020000); }
DI float asf(unsigned u) { return __builtin_bit_cast(float, u); }
DI unsigned asu(float f) { return __builtin_bit_cast(unsigned, f); }
DI unsigned cvtpk(float a, float b) { f32x2 v = {a, b}; return __builtin_bit_cast(unsigned, __builtin_convertvector(v, bf16x2)); }
DI float bflo(unsigned u) { return asf(u << 16); }
DI float bfhi(unsigned u) { return asf(u & 0xffff0000u); }
DI float rbf(float a) { return bflo(cvtpk(a, a)); }
DI f32x4 mfma(v4u a, v4u b, f32x4 c) { return __builtin_amdgcn_mfma_f32_16x16x32_bf16(__builtin_bit_cast(bf16x8, a), __builtin_bit_cast(bf16x8, b), c, 0, 0, 0); }
template <int CTRL> DI float dppf(float x) { return asf(__builtin_amdgcn_update_dpp(0u, asu(x), CTRL, 0xf, 0xf, true)); }
constexpr int ROR8 = 0x128, ROR4 = 0x124, XOR1 = 0xB1, XOR2 = 0x4E;
DI float add16(float a, float b) { auto s = __builtin_amdgcn_permlane16_swap(asu(a), asu(b), false, false); return asf(s[0]) + asf(s[1]); }
DI float add32(float a, float b) { auto s = __builtin_amdgcn_permlane32_swap(asu(a), asu(b), false, false); return asf(s[0]) + asf(s[1]); }
DI float max16(float a) { auto s = __builtin_amdgcn_permlane16_swap(asu(a), asu(a), false, false); return fmaxf(asf(s[0]), asf(s[1])); }
DI float max32(float a) { auto s = __builtin_amdgcn_permlane32_swap(asu(a), asu(a), false, false); return fmaxf(asf(s[0]), asf(s[1])); }
DI float wave_sum(float a) { a += dppf<XOR1>(a); a += dppf<XOR2>(a); a += dppf<ROR8>(a); a += dppf<ROR4>(a); a = add16(a, a); return add32(a, a); }
DI float wave_max(float a) { a = fmaxf(a, dppf<XOR1>(a)); a = fmaxf(a, dppf<XOR2>(a)); a = fmaxf(a, dppf<ROR8>(a)); a = fmaxf(a, dppf<ROR4>(a)); return max32(max16(a)); }
DI float rs4(f32x4 v) { return add16(add32(v.x, v.z), add32(v.y, v.w)); }
DI u64 rtc() { return __builtin_amdgcn_s_memrealtime(); }
#define SB() __builtin_amdgcn_sched_barrier(0)
#define CFENCE() asm volatile("" ::: "memory")
template <int N> DI void wait_vm() { asm volatile("s_waitcnt vmcnt(%0)" ::"i"(N) : "memory"); }
DI void wait_lds() { asm volatile("s_waitcnt lgkmcnt(0)" ::: "memory"); }
DI void barrier() { asm volatile("s_waitcnt lgkmcnt(0)\n\ts_barrier" ::: "memory"); SB(); }

struct Tag { unsigned t, b0, b1, b2, b3; };
DI Tag mktag(unsigned t) { return Tag{t, t & 255u, (t >> 8) & 255u, (t >> 16) & 255u, t >> 24}; }
DI v4u enc(f32x4 v, const Tag& g) { return v4u{(asu(v.x) & ~255u) | g.b0, (asu(v.y) & ~255u) | g.b1, (asu(v.z) & ~255u) | g.b2, (asu(v.w) & ~255u) | g.b3}; }
DI f32x4 dec(v4u v, const Tag& g, unsigned& bad) {
  v.x ^= g.b0; v.y ^= g.b1; v.z ^= g.b2; v.w ^= g.b3;
  bad |= (v.x | v.y) | (v.z | v.w);
  return __builtin_bit_cast(f32x4, v);
}
DI bool all_ok(unsigned bad) { return __builtin_amdgcn_ballot_w64((bad & 255u) != 0u) == 0ull; }
DI v4u ld_sc1(rsrc_t r, int vo, int so) { return __builtin_amdgcn_raw_buffer_load_b128(r, vo, so, AUX_SC1); }
DI void st_sc1(v4u v, rsrc_t r, int vo, int so) { __builtin_amdgcn_raw_buffer_store_b128(v, r, vo, so, AUX_SC1); }
DI v4u ld_nt(rsrc_t r, int vo, int so) { return __builtin_amdgcn_raw_buffer_load_b128(r, vo, so, AUX_NT); }
template <int N, int SLEEP = 0> DI void gather(rsrc_t r, int vo, int so, int stride, const Tag& g, f32x4 (&o)[N], int cap) {
#pragma unroll 1
  for (int spin = 0; spin < cap; ++spin) {
    CFENCE();
    v4u v[N];
#pragma unroll
    for (int i = 0; i < N; ++i) v[i] = ld_sc1(r, vo, so + i * stride);
    unsigned bad = 0;
#pragma unroll
    for (int i = 0; i < N; ++i) o[i] = dec(v[i], g, bad);
    if (all_ok(bad)) break;
    if (SLEEP) __builtin_amdgcn_s_sleep(SLEEP);
  }
}

DI v4u tr1(const v4u (&a)[8], int e) {
  const unsigned sel = (e & 1) ? 0x07060302u : 0x05040100u;
  return v4u{__builtin_amdgcn_perm(a[1][e >> 1], a[0][e >> 1], sel), __builtin_amdgcn_perm(a[3][e >> 1], a[2][e >> 1], sel),
             __builtin_amdgcn_perm(a[5][e >> 1], a[4][e >> 1], sel), __builtin_amdgcn_perm(a[7][e >> 1], a[6][e >> 1], sel)};
}
DI void tr8(v4u (&a)[8]) {
  v4u t[8];
#pragma unroll
  for (int e = 0; e < 8; ++e) t[e] = tr1(a, e);
#pragma unroll
  for (int e = 0; e < 8; ++e) { asm volatile("" : "+v"(t[e])); a[e] = t[e]; }
}
DI v4u buildX(float v) {
  const unsigned pk = cvtpk(v, dppf<0x102>(v)), one = 0x3f803f80u;
  return v4u{(unsigned)__builtin_amdgcn_update_dpp(one, pk, 0xE4, 0xf, 0x1, false), (unsigned)__builtin_amdgcn_update_dpp(one, pk, 0x104, 0xf, 0x1, false),
             (unsigned)__builtin_amdgcn_update_dpp(one, pk, 0x108, 0xf, 0x1, false), (unsigned)__builtin_amdgcn_update_dpp(one, pk, 0x10C, 0xf, 0x1, false)};
}
DI f32x4 fold(const f32x4 (&acc)[8], bool s) {
  f32x4 r;
#pragma unroll
  for (int i = 0; i < 4; ++i) r[i] = (s ? acc[4 + i][1] : acc[i][0]) + dppf<ROR8>(s ? acc[i][1] : acc[4 + i][0]);
  return r;
}

#define STAMP(i) do { u64 t_; asm volatile("s_memrealtime %0\n\ts_waitcnt lgkmcnt(0)" : "=s"(t_)::"memory"); stamp[i] = t_; } while (0)
#define STAMP_ARG , u64 (&stamp)[12]
#define STAMP_PASS , stamp

struct Attn {
  rsrc_t rk, rv;
  int base;
  unsigned ring;
  int vk[4], vn;
  LAS unsigned char* L;
  int lane, w;
  unsigned t0;
};
static_assert(PD >= 2 && PD <= 9, "a K tile is read two units after its issue");
template <int J> DI void issueK1(const Attn& A, int t) {
  asm volatile("s_mov_b32 m0, %0\n\ts_nop 0\n\tbuffer_load_dwordx4 %1, %2, %3 offen offset:%4 nt lds"
               ::"s"(A.ring + (t & 3) * 8192 + (J >> 2) * 4096), "v"(A.vk[J & 3]), "s"(A.rk), "s"(A.base + t * TSTR + (J >> 2) * 4096), "i"((J & 3) * 1024) : "memory");
}
template <int OFF> DI void clock_wait(unsigned t0) {
  if (TAU)
    asm volatile("1:\n\ts_memrealtime vcc\n\ts_waitcnt lgkmcnt(0)\n\ts_sub_u32 vcc_lo, vcc_lo, %0\n\ts_cmp_lt_u32 vcc_lo, %1\n\ts_cbranch_scc1 1b"
                 ::"s"(t0), "i"(OFF) : "memory", "scc", "vcc");
}
template <int N> DI void clock_pace(const Attn& A) { clock_wait<TOFF + N * TAU / 16>(A.t0); }
template <int J> struct Ic { static constexpr int v = J; };
template <int U, int J0 = 0, class FI, class FC> DI void unit(const Attn& A, FI issue, FC comp) {
#define STEP(J) if (J >= J0) { if (U >= 0 && (J & 3) == 0) clock_pace<U * 8 + J>(A); wait_vm<PD - 1>(); issue(Ic<J>{}); SB(); comp(Ic<J>{}); SB(); }
  STEP(0) STEP(1) STEP(2) STEP(3) STEP(4) STEP(5) STEP(6) STEP(7)
#undef STEP
}
DI void scoreK(const Attn& A, int t, int u, const v4u (&q)[4], const int (&ra)[4], unsigned (&S)[4], float& mx) {
  f32x4 c = {0.f, 0.f, 0.f, 0.f};
#pragma unroll
  for (int s = 0; s < 4; ++s) c = mfma(*(const LAS v4u*)(A.L + ra[s] + (t & 3) * 8192 + u * 4096), q[s], c);
  mx = fmaxf(fmaxf(mx, c.x), fmaxf(c.y, fmaxf(c.z, c.w)));
  const unsigned a = cvtpk(c.x, c.y), b = cvtpk(c.z, c.w);
  S[2 * u] = cvtpk(bflo(a) * SCALE, bfhi(a) * SCALE);
  S[2 * u + 1] = cvtpk(bflo(b) * SCALE, bfhi(b) * SCALE);
}
DI void expP(unsigned (&S)[4], float m2, float& l) {
#pragma unroll
  for (int i = 0; i < 4; ++i) {
    const float a = __builtin_amdgcn_exp2f(fmaf(bflo(S[i]), LOG2E, -m2)), b = __builtin_amdgcn_exp2f(fmaf(bfhi(S[i]), LOG2E, -m2));
    l += a + b;
    S[i] = cvtpk(a, b);
  }
}
DI void pv(const v4u (&s)[8], const unsigned (&P)[4], f32x4 (&acc)[8], int h) {
  const v4u p = {P[0], P[1], P[2], P[3]};
#pragma unroll
  for (int e = 4 * h; e < 4 * h + 4; ++e) acc[e] = mfma(tr1(s, e), p, acc[e]);
}
DI bool last_of_four(LAS unsigned char* cnt) {
  wait_lds();
  unsigned old = 0;
  if (__builtin_amdgcn_mbcnt_hi(~0u, __builtin_amdgcn_mbcnt_lo(~0u, 0u)) == 0) old = __hip_atomic_fetch_add((LAS unsigned*)cnt, 1u, __ATOMIC_RELAXED, __HIP_MEMORY_SCOPE_WORKGROUP);
  const bool last = __builtin_amdgcn_readfirstlane(old) == 3u;
  CFENCE();
  return last;
}
DI void put_ml(const Attn& A, float m2, float l) {
  l = add32(add16(l, l), add16(l, l));
  if (A.lane < 8) *(LAS f32x2*)(A.L + L_ML + (A.w * 8 + A.lane) * 8) = f32x2{m2, l};
  wait_lds();
  if (A.lane == 0) *(volatile LAS unsigned*)(A.L + L_FLAG + A.w * 4) = 1u;
}
DI void ml_duty(LAS unsigned char* L, rsrc_t rw, int lane, int mlo, unsigned tag) {
#pragma unroll 1
  for (int spin = 0; spin < SPIN_CAP; ++spin) {
    const v4u f = *(volatile LAS v4u*)(L + L_FLAG);
    if (f.x & f.y & f.z & f.w) break;
    __builtin_amdgcn_s_sleep(8);
  }
  CFENCE();
  const f32x2 ml = *(const LAS f32x2*)(L + L_ML + (lane & 31) * 8);
  const float M = max16(fmaxf(ml.x, dppf<ROR8>(ml.x)));
  const float f = __builtin_amdgcn_exp2f(ml.x - M);
  float s = f * ml.y;
  s += dppf<ROR8>(s);
  s = add16(s, s);
  if (lane < 32) *(LAS float*)(L + L_F + lane * 4) = f;
  if (lane < 8) st_sc1(v4u{asu(M), tag, asu(s), tag}, rw, lane * 1024, mlo);
  wait_lds();
}

template <int NT> DI void attn_wave(Attn& A, v4u (&G)[8], rsrc_t rg, int gvo, int gso, unsigned& tag STAMP_ARG) {
  const int g = A.lane >> 4, i = A.lane & 15;
  v4u V0[8], V1[8], V2[8], V3[8];
  unsigned S[NT][4];
  auto none = [](auto) {};
#define IK(t) [&](auto j) { issueK1<decltype(j)::v>(A, t); }
#define IV(t, R) [&](auto j) { constexpr int J = decltype(j)::v; R[J] = ld_nt(A.rv, A.vn + (J & 3) * 1024, A.base + (t) * TSTR + (J >> 2) * 4096); }
#define CO(...) [&](auto j) { constexpr int J = decltype(j)::v; __VA_ARGS__ }
  unit<-1, 4>(A, IK(0), none);
  if (NT > 1) { unit<-1>(A, IK(1), none); unit<-1>(A, IK(2), none); }
  unit<-1>(A, IV(0, V0), none);
  if (NT > 1) {
    unit<-1>(A, IV(1, V1), none);
    asm volatile("s_movk_i32 vcc_hi, 0x7fff\n1:\n\tds_read_b32 %0, %1\n\ts_waitcnt lgkmcnt(0)\n\tv_readfirstlane_b32 vcc_lo, %0\n\ts_sub_u32 vcc_hi, vcc_hi, 1\n\ts_cbranch_scc1 2f\n\t"
                 "s_cmp_eq_u32 vcc_lo, 4\n\ts_cbranch_scc0 1b\n2:" : "=&v"(S[0][0]) : "v"((unsigned)(size_t)A.L + L_QFLAG) : "memory", "scc", "vcc");
    unit<-1>(A, IK(3), none);
  }
  STAMP(1);
  barrier();
  STAMP(2);
  v4u q[4]; int ra[4];
#pragma unroll
  for (int s = 0; s < 4; ++s) {
    q[s] = *(const LAS v4u*)(A.L + L_Q + (i & 7) * 256 + s * 64 + g * 16);
    ra[s] = A.w * 32768 + (4 * (i & 3) + (i >> 2)) * 256 + (((4 * s + g) ^ i) << 4);
  }
  { const v2u tt = *(const LAS v2u*)(A.L + L_TAG); tag = __builtin_amdgcn_readfirstlane(tt.x); A.t0 = __builtin_amdgcn_readfirstlane(tt.y) + (STAG ? A.w * (TAU / 16) : 0); }
  float mx = -INFINITY, l = 0.f, m2;
  f32x4 acc[8];
#pragma unroll
  for (int e = 0; e < 8; ++e) acc[e] = f32x4{0.f, 0.f, 0.f, 0.f};
#define SK(t, u) scoreK(A, t, u, q, ra, S[(t) % NT], mx)
#define EP(t) expP(S[(t) % NT], m2, l)
#define PV(t, R, h) pv(R, S[(t) % NT], acc, h)
  if (NT == 1) {
    wait_vm<8>();
    SK(0, 0); SK(0, 1);
    m2 = rbf(rbf(max32(max16(mx))) * SCALE) * LOG2E;
    EP(0);
    put_ml(A, m2, l);
    PV(0, V0, 0); PV(0, V0, 1);
  } else {
    wait_vm<24>();
    SK(0, 0);
    SB();
    unit<6>(A, IK(4), CO(if (J < 5) SK((J + 1) >> 1, (J + 1) & 1);));
    unit<7>(A, IK(5), CO(if (J < 2) SK(3, J);));
    unit<8>(A, IK(6), CO(if (J < 2) SK(4, J);));
    unit<9>(A, IK(7), CO(if (J < 2) SK(5, J);));
    unit<10>(A, IK(8), CO(if (J < 2) SK(6, J);));
    unit<11>(A, IV(2, V2), CO(if (J < 2) SK(7, J);));
    unit<12>(A, IV(3, V3), CO(if (J < 2) SK(8, J);
                              if (J == 2) { m2 = rbf(rbf(max32(max16(mx))) * SCALE) * LOG2E; EP(0); }
                              if (J == 3 || J == 4) PV(0, V0, J - 3);
                              if (J == 5) EP(1);
                              if (J >= 6) PV(1, V1, J - 6);));
    unit<13>(A, IV(4, V0), CO(if (J == 0) EP(2);
                              if (J == 1 || J == 2) PV(2, V2, J - 1);
                              if (J >= 3) EP(J);));
    unit<14>(A, IV(5, V1), CO(if (J == 0) EP(8);
                              if (J == 1) put_ml(A, m2, l);
                              if (J == 2 || J == 3) PV(3, V3, J - 2);));
    STAMP(3);
    unit<15>(A, IV(6, V2), CO(if (J < 2) PV(4, V0, J);));
    unit<16>(A, IV(7, V3), CO(if (J < 2) PV(5, V1, J);));
    unit<17>(A, IV(8, V0), CO(if (J < 2) PV(6, V2, J);));
    PV(7, V3, 0); PV(7, V3, 1);
    SB();
    if (GEARLY) clock_pace<144>(A);
#pragma unroll
    for (int j = 0; j < GEARLY; ++j) { wait_vm<PD - 1>(); G[j] = ld_nt(rg, gvo, gso + j * 8 * 7168); SB(); }
    PV(8, V0, 0); PV(8, V0, 1);
  }
  STAMP(4);
  if (i < 8) {
#pragma unroll
    for (int e = 0; e < 8; ++e) *(LAS f32x4*)(A.L + A.w * 32768 + i * OSTR + g * 128 + e * 16) = acc[e];
  }
}
DI void publish_o(LAS unsigned char* L, rsrc_t rw, int tid, int grp, int prod, const Tag& T) {
  const int h = tid >> 5, c = tid & 31;
  f32x4 o = {0.f, 0.f, 0.f, 0.f};
#pragma unroll
  for (int w = 0; w < 4; ++w) o += *(const LAS float*)(L + L_F + (w * 8 + h) * 4) * *(const LAS f32x4*)(L + w * 32768 + h * OSTR + c * 16);
  st_sc1(enc(o, T), rw, h * 32768 + c * 16, WS_OP + grp * 8 * 32768 + prod * 512);
}

DI void helper_loads(const Params& p, LAS unsigned char* L, rsrc_t rw, int b, int hw, int lane) {
  const int g = lane >> 4, n = lane & 15, cb = b >> 2, kq = b & 3;
  const rsrc_t rx = mk(p.x, 2048), rq = mk(p.wq, 8 << 20);
  asm volatile("s_mov_b32 m0, %0\n\ts_nop 0\n\tbuffer_load_dwordx4 %1, %2, 0 offen lds\n\tbuffer_load_dwordx4 %1, %2, 0 offen offset:1024 lds\n\t"
               "s_add_u32 m0, m0, 0x800\n\ts_nop 0\n\tbuffer_load_dword off, %3, 0 sc1 lds"
               ::"s"((unsigned)(size_t)L + L_X + hw * 2304), "v"(lane * 16), "s"(rx), "s"(rw) : "memory", "scc");
  const int vo = (4 * (n >> 3) + g) * 8192 + (n & 7) * 16;
#pragma unroll
  for (int j = 0; j < 8; ++j)
    asm volatile("s_mov_b32 m0, %0\n\ts_nop 0\n\tbuffer_load_dwordx4 %1, %2, %3 offen nt lds"
                 ::"s"((unsigned)(size_t)L + hw * 32768 + 24576 + j * 1024), "v"(vo), "s"(rq), "s"((kq * 256 + hw * 64 + j * 8) * 8192 + cb * 128) : "memory");
}
DI void helper_wave(LAS unsigned char* L, rsrc_t rw, int b, int hw, int lane, int grp, u64 t0, unsigned& tag STAMP_ARG) {
  Tag T;
  const int g = lane >> 4, n = lane & 15, cb = b >> 2, kq = b & 3;
  wait_vm<8>();
  const LAS unsigned char* X0 = L + L_X + hw * 2304;
  const v4u xa = *(const LAS v4u*)(X0 + lane * 16), xb = *(const LAS v4u*)(X0 + 1024 + lane * 16);
  const unsigned xo = *(const LAS unsigned short*)(X0 + kq * 512 + hw * 128 + (4 * n + g) * 2), e0 = *(const LAS unsigned*)(X0 + 2048);
  {
    float s = 0.f;
#pragma unroll
    for (int k = 0; k < 4; ++k) s += (bflo(xa[k]) + bfhi(xa[k])) + (bflo(xb[k]) + bfhi(xb[k]));
    const float mean = wave_sum(s) * (1.f / 1024.f);
    float v = 0.f;
#pragma unroll
    for (int k = 0; k < 4; ++k) {
      const float d0 = bflo(xa[k]) - mean, d1 = bfhi(xa[k]) - mean, d2 = bflo(xb[k]) - mean, d3 = bfhi(xb[k]) - mean;
      v += (d0 * d0 + d1 * d1) + (d2 * d2 + d3 * d3);
    }
    const float var = wave_sum(v) * (1.f / 1024.f);
    const float mb = rbf(mean), sd = rbf(sqrtf(rbf(rbf(var) + EPS)));
    const v4u X = buildX(rbf(asf((unsigned)xo << 16) - mb) / sd);
    const unsigned e = __builtin_amdgcn_readfirstlane(e0);
    T = mktag(((e & 0x7f7f7f7fu) + 0x01010101u) ^ (e & 0x80808080u));
    SB();
    wait_vm<0>();
    v4u W[8];
#pragma unroll
    for (int j = 0; j < 8; ++j) W[j] = *(const LAS v4u*)(L + hw * 32768 + 24576 + j * 1024 + lane * 16);
    f32x4 acc[8];
#pragma unroll
    for (int e = 0; e < 8; ++e) acc[e] = mfma(X, tr1(W, e), f32x4{0.f, 0.f, 0.f, 0.f});
    const f32x4 r = fold(acc, n >> 3);
    if (lane < 16) *(LAS f32x4*)(L + L_RED + hw * 256 + ((n & 7) * 8 + (n >> 3) * 4) * 4) = r;
    if (last_of_four(L + L_QFLAG)) {
      const LAS float* rr = (const LAS float*)(L + L_RED + lane * 4);
      __builtin_amdgcn_raw_buffer_store_b64(v2u{asu((rr[0] + rr[64]) + (rr[128] + rr[192])), T.t}, rw, lane * 8, WS_QP + kq * 32768 + cb * 512, AUX_SC1);
      if (TAU && b == 0 && lane == 0) __builtin_amdgcn_raw_buffer_store_b64(v2u{(unsigned)t0, T.t}, rw, 0, WS_T0, AUX_SC1);
    }
  }
  STAMP(1);
  while ((long long)(rtc() - t0) < QDELAY) __builtin_amdgcn_s_sleep(2);
  {
    v4u v[8];
    v2u tz = {0u, T.t};
#pragma unroll 1
    for (int spin = 0; spin < SPIN_CAP; ++spin) {
      CFENCE();
      if (TAU) tz = __builtin_amdgcn_raw_buffer_load_b64(rw, 0, WS_T0, AUX_SC1);
#pragma unroll
      for (int i = 0; i < 8; ++i) v[i] = ld_sc1(rw, lane * 16 + (i & 1) * 1024, WS_QP + (i >> 1) * 32768 + grp * 8192 + hw * 2048);
      unsigned bad = tz.y ^ T.t;
#pragma unroll
      for (int i = 0; i < 8; ++i) bad |= (v[i].y ^ T.t) | (v[i].w ^ T.t);
      if (__builtin_amdgcn_ballot_w64(bad != 0u) == 0ull) break;
      __builtin_amdgcn_s_sleep(QSLEEP);
    }
    if (hw == 0 && lane == 0) *(LAS v2u*)(L + L_TAG) = v2u{T.t, tz.x};
#pragma unroll
    for (int a = 0; a < 2; ++a)
      *(LAS unsigned*)(L + L_Q + hw * 512 + a * 256 + lane * 4) =
          cvtpk((asf(v[a].x) + asf(v[2 + a].x)) + (asf(v[4 + a].x) + asf(v[6 + a].x)), (asf(v[a].z) + asf(v[2 + a].z)) + (asf(v[4 + a].z) + asf(v[6 + a].z)));
  }
  tag = T.t;
  STAMP(2);
  barrier();
}

DI void ffn_tail(const Params& p, LAS unsigned char* L, rsrc_t rw, int cg, int kq, int w, int lane, int grp, int prod, const Tag& T, v4u (&G)[8], unsigned t0 STAMP_ARG) {
  const int g = lane >> 4, n = lane & 15, rec = cg * 4 + kq;
  const int wbytes = (ABL & 2) ? 0 : 7 << 20;
  const rsrc_t rg = mk(p.wg, wbytes), ru = mk(p.wu, wbytes), rd = mk(p.wd, wbytes);
  v4u U[8], Dn[8];
  const int rsel = 4 * (n >> 3) + g, vo = rsel * 7168 + (n & 7) * 16, so = (kq * 256 + w * 64) * 7168 + cg * 128;
  barrier();
  publish_o(L, rw, w * 64 + lane, grp, prod, T);
  barrier();
  const unsigned tp = (unsigned)rtc();
  STAMP(5);
  constexpr int T144 = TOFF + 144 * TAU / 16;
  const unsigned xo = *(const LAS unsigned short*)(L + L_X + kq * 512 + w * 128 + (4 * n + g) * 2);
  if (PREP) {
    if (w == 0) asm volatile("s_mov_b32 m0, %0\n\ts_nop 0\n\tbuffer_load_dword %1, %2, 0 offen sc1 lds"
                             ::"s"((unsigned)(size_t)L + L_SINK), "v"(lane < 32 ? WS_GU + cg * 4096 + lane * 128 : WS_ST + cg * 512 + (lane & 3) * 128), "s"(rw) : "memory");
  }
#pragma unroll
  for (int j = GEARLY; j < 8; ++j) G[j] = ld_nt(rg, vo, so + j * 8 * 7168);
  SB();
  clock_wait<T144 + 8 * TAU2 / 16>(t0);
#pragma unroll
  for (int j = 0; j < 4; ++j) U[j] = ld_nt(ru, vo, so + j * 8 * 7168);
  SB();
  clock_wait<T144 + 12 * TAU2 / 16>(t0);
#pragma unroll
  for (int j = 4; j < 8; ++j) U[j] = ld_nt(ru, vo, so + j * 8 * 7168);
  SB();
  tr8(G);
  SB();
#define LD_D(j) Dn[j] = ld_nt(rd, rsel * 2048 + (n & 7) * 16, (cg * 64 + (j) * 8) * 2048 + kq * 512 + w * 128)
  if (DPOS == 0 || DPOS == 2) {
    clock_wait<T144 + 16 * TAU2 / 16>(t0);
#pragma unroll
    for (int j = 0; j < 4; ++j) LD_D(j);
    SB();
  }
  if (DPOS == 0) {
    clock_wait<T144 + 20 * TAU2 / 16>(t0);
#pragma unroll
    for (int j = 4; j < 8; ++j) LD_D(j);
    SB();
  }
  tr8(U);
  SB();
  if (DPOS == 0) { tr8(Dn); SB(); }
  STAMP(6);
  clock_wait<THP>(tp);
  STAMP(7);
  f32x4 hp[8];
  gather<8, HSLEEP>(rw, g * 4096 + n * 16, WS_HP + kq * 1024 + w * 256, 16384, T, hp, SPIN_CAP);
  STAMP(8);
  if (DPOS) {
#pragma unroll
    for (int j = DPOS == 2 ? 4 : 0; j < 8; ++j) LD_D(j);
    SB();
  }
  const float h = rbf(asf(xo << 16) + rbf(rs4(((hp[0] + hp[1]) + (hp[2] + hp[3])) + ((hp[4] + hp[5]) + (hp[6] + hp[7])))));
  const v4u X = buildX(h);
  const f32x4 z = {0.f, 0.f, 0.f, 0.f};
  const f32x4 gram = mfma(X, X, z);
  f32x4 ag[8], au[8];
#pragma unroll
  for (int e = 0; e < 8; ++e) ag[e] = mfma(X, G[e], z);
#pragma unroll
  for (int e = 0; e < 8; ++e) au[e] = mfma(X, U[e], z);
  if (lane < 8) *(LAS float*)(L + L_ST + lane * 16 + w * 4) = lane == 0 ? gram.x : lane == 1 ? gram.y : gram.x + gram.y;
  const f32x4 rgv = fold(ag, n >> 3), ruv = fold(au, n >> 3);
  if (lane < 32) {
#pragma unroll
    for (int i = 0; i < 4; ++i) *(LAS f32x2*)(L + L_RED + w * 1024 + ((n & 7) * 8 + (n >> 3) * 4 + i) * 16 + g * 8) = f32x2{rgv[i], ruv[i]};
  }
  barrier();
  if (w == 0) {
    const LAS f32x4* r = (const LAS f32x4*)(L + L_RED + lane * 16);
    const LAS f32x4* sp = (const LAS f32x4*)(L + L_ST);
    const f32x4 own = (r[0] + r[64]) + (r[128] + r[192]), sa = sp[0] + sp[1], sb = sp[4];
    st_sc1(enc(own, T), rw, lane * 16, WS_GU + rec * 1024);
    if (lane < 8) st_sc1(v4u{asu((sb.x + sb.y) + (sb.z + sb.w)), T.t, asu((sa.x + sa.y) + (sa.z + sa.w)), T.t}, rw, lane * 16, WS_ST + rec * 128);
    STAMP(9);
    if (CDELAY) __builtin_amdgcn_s_sleep(CDELAY);
    f32x4 gu[4]; v4u st;
#pragma unroll 1
    for (int spin = 0; spin < SPIN_CAP; ++spin) {
      CFENCE();
      v4u v[4];
#pragma unroll
      for (int i = 0; i < 4; ++i) v[i] = ld_sc1(rw, lane * 16 + i * 1024, WS_GU + cg * 4096);
      st = ld_sc1(rw, (lane & 3) * 128, WS_ST + cg * 512);
      unsigned bad = 0;
#pragma unroll
      for (int i = 0; i < 4; ++i) gu[i] = dec(v[i], T, bad);
      if (__builtin_amdgcn_ballot_w64(((bad & 255u) | (st.y ^ T.t) | (st.w ^ T.t)) != 0u) == 0ull) break;
    }
    STAMP(10);
    float s1 = asf(st.x), s2 = asf(st.z);
    s1 += dppf<XOR1>(s1); s2 += dppf<XOR1>(s2); s1 += dppf<XOR2>(s1); s2 += dppf<XOR2>(s2);
    const float mean = s1 * (1.f / 1024.f), var = fmaxf(fmaf(-mean, mean, s2 * (1.f / 1024.f)), 0.f);
    const float mb = rbf(mean), rinv = __builtin_amdgcn_rcpf(rbf(__builtin_amdgcn_sqrtf(rbf(rbf(var) + EPS))));
    const f32x4 t = (gu[0] + gu[1]) + (gu[2] + gu[3]);
    const unsigned gub = cvtpk(fmaf(-mb, t.z, t.x) * rinv, fmaf(-mb, t.w, t.y) * rinv);
    const float gv = bflo(gub), uv = bfhi(gub);
    const float y = gv * fmaf(gv * gv, 0.044715f, 1.f) * (-2.f * 0.7978845608028654f * LOG2E);
    const float a = rbf(gv * __builtin_amdgcn_rcpf(1.f + __builtin_amdgcn_exp2f(y))) * uv;
    *(LAS unsigned short*)(L + L_ACT + (lane & 7) * 16 + (lane >> 3) * 2) = (unsigned short)cvtpk(a, a);
  } else {
    if (rec == 0 && w == 2 && lane == 0) __builtin_amdgcn_raw_buffer_store_b32(T.t, rw, 0, WS_EPOCH, AUX_SC1);
  }
  if (DPOS) { tr8(Dn); SB(); }
  barrier();
  const v4u Xa = *(const LAS v4u*)(L + L_ACT + ((n & 1) * 4 + g) * 16);
  f32x4 ad[8];
#pragma unroll
  for (int e = 0; e < 8; ++e) ad[e] = mfma(Xa, Dn[e], z);
  const f32x4 r = fold(ad, n >> 3);
  if (lane < 16) st_sc1(enc(r, T), rw, ((n & 7) >> 2) * (56 * 128) + (n & 3) * 32 + (n >> 3) * 16, WS_DP + ((kq * 8 + w * 2) * 56 + cg) * 128);
  STAMP(11);
}

DI void wo_tail(const Params& p, LAS unsigned char* L, rsrc_t rw, int j, int w, int lane, const Tag& T0 STAMP_ARG) {
  const int g = lane >> 4, n = lane & 15;
  const rsrc_t ro = mk(p.wo, (ABL & 1) ? 0 : 8 << 20), rx = mk(p.x, 2048), rout = mk(p.out, 2048);
  v4u W[4][8];
#pragma unroll
  for (int t = 0; t < 4; ++t) {
#pragma unroll
    for (int jj = 0; jj < 8; ++jj) {
      wait_vm<WD - 1>();
      W[t][jj] = ld_nt(ro, g * 32 * 2048 + n * 16, (j * 128 + 8 * (jj & 3) + 2 * t + (jj >> 2)) * 2048 + w * 256);
      SB();
    }
    if (t > 0) { tr8(W[t - 1]); SB(); }
  }
  tr8(W[3]);
  SB();
  const unsigned short xo = __builtin_amdgcn_raw_buffer_load_b16(rx, (4 * (lane & 7) + g) * 2, j * 64, 0);
  STAMP(6);
  if (ABL & 4) { while ((long long)(rtc() - stamp[0]) < 2600) __builtin_amdgcn_s_sleep(32); }
  int dry = DRY;
  asm volatile("" : "+s"(dry));
#pragma unroll 1
  for (; dry >= 0; --dry) {
  const int cap = dry ? 1 : SPIN_CAP;
  const Tag T = mktag(dry ? ~T0.t : T0.t);
  v4u ml;
#pragma unroll 1
  for (int spin = 0; spin < cap; ++spin) {
    CFENCE();
    ml = ld_sc1(rw, lane * 16, WS_ML + j * 1024);
    if (__builtin_amdgcn_ballot_w64(((ml.y ^ T.t) | (ml.w ^ T.t)) != 0u) == 0ull) break;
    __builtin_amdgcn_s_sleep(16);
  }
  const float wp = __builtin_amdgcn_exp2f(asf(ml.x) - wave_max(asf(ml.x)));
  const float cp = wp / wave_sum(wp * asf(ml.z));
  float c[4];
#pragma unroll
  for (int i = 0; i < 4; ++i) {
    const float a = asf(__builtin_amdgcn_readlane(asu(cp), 8 * w + 2 * i)), bb = asf(__builtin_amdgcn_readlane(asu(cp), 8 * w + 2 * i + 1));
    c[i] = lane < 32 ? a : bb;
  }
  STAMP(7);
  f32x4 op[4];
  gather<4>(rw, lane * 16, WS_OP + j * 32768 + w * 4096, 1024, T, op, cap);
  const f32x4 o4 = (c[0] * op[0] + c[1] * op[1]) + (c[2] * op[2] + c[3] * op[3]);
  *(LAS f32x2*)(L + L_RED + w * 512 + (lane & 31) * 16 + (lane >> 5) * 8) = f32x2{add32(o4.x, o4.z), add32(o4.y, o4.w)};
  barrier();
  STAMP(8);
  const LAS f32x2* r = (const LAS f32x2*)(L + L_RED + lane * 8);
  const f32x2 o2 = ((r[0] + r[64]) + (r[128] + r[192])) + ((r[256] + r[320]) + (r[384] + r[448]));
  const unsigned ob = cvtpk(o2.x, o2.y);
  f32x4 acc[8];
#pragma unroll
  for (int e = 0; e < 8; ++e) acc[e] = f32x4{0.f, 0.f, 0.f, 0.f};
#define BC(k) (unsigned)__builtin_amdgcn_update_dpp(0u, ob, 0x150 + (k), 0xf, 0xf, true)
  const v4u X[4] = {v4u{BC(0), BC(1), BC(2), BC(3)}, v4u{BC(4), BC(5), BC(6), BC(7)}, v4u{BC(8), BC(9), BC(10), BC(11)}, v4u{BC(12), BC(13), BC(14), BC(15)}};
#undef BC
#pragma unroll
  for (int t = 0; t < 4; ++t)
#pragma unroll
    for (int e = 0; e < 8; ++e) acc[e] = mfma(X[t], W[t][e], acc[e]);
  const bool hi = g & 1;
  const f32x4 y = {hi ? acc[4].x : acc[0].x, hi ? acc[5].x : acc[1].x, hi ? acc[6].x : acc[2].x, hi ? acc[7].x : acc[3].x};
  if (lane < 32) st_sc1(enc(y, T), rw, n * 32 + g * 16, WS_HP + j * 4096 + w * 512);
  STAMP(9);
  if (w == 0) {
    f32x4 hp[4];
    gather<4>(rw, (lane >> 3) * 4096 + (lane & 7) * 16, WS_HP + j * 128, 32768, T, hp, cap);
    float hs = rs4((hp[0] + hp[1]) + (hp[2] + hp[3]));
    hs += dppf<ROR8>(hs);
    const float h = rbf(asf((unsigned)xo << 16) + rbf(hs));
    STAMP(10);
    f32x4 dp[7];
    gather<7>(rw, lane * 16, WS_DP + j * 56 * 128, 1024, T, dp, cap);
    float ds = rs4(((dp[0] + dp[1]) + (dp[2] + dp[3])) + ((dp[4] + dp[5]) + dp[6]));
    ds += dppf<ROR8>(ds);
    const float o = h + rbf(ds);
    if ((lane & 8) == 0) __builtin_amdgcn_raw_buffer_store_b16((unsigned short)cvtpk(o, o), rout, (4 * (lane & 7) + g) * 2, j * 64, 0);
    STAMP(11);
  }
  if (dry) { wait_vm<0>(); barrier(); }
  }
}

__global__ void __launch_bounds__(512, 2) decode_block_kernel(Params p) {
  __shared__ __attribute__((aligned(16))) unsigned char smem[L_END];
  LAS unsigned char* L = (LAS unsigned char*)smem;
  const int tid = threadIdx.x, lane = tid & 63, w = __builtin_amdgcn_readfirstlane(tid >> 6), b = blockIdx.x;
  u64 stamp[12];
  for (int i = 0; i < 12; ++i) stamp[i] = 0;
  STAMP(0);
  const rsrc_t rw = mk(p.ws, WS_END);
  const int w4 = w & 3;
  int r0 = w >> 2, r1 = r0, r2 = r0, r3 = r0;
  asm volatile("" : "+s"(r1));
  asm volatile("" : "+s"(r2));
  asm volatile("" : "+s"(r3));
  const u64 t0 = rtc();
  if (r0 == 0) {
    u64 pc; int len;
    asm volatile("s_getpc_b64 %0\n.Lcode_from:\n\ts_mov_b32 %1, .Lcode_to-.Lcode_from" : "=s"(pc), "=s"(len));
    asm volatile("s_mov_b32 m0, %0\n\ts_nop 0\n\tbuffer_load_dword %1, %2, %3 offen lds"
                 ::"s"((unsigned)(size_t)L + L_SINK), "v"((lane & 3) * 128), "s"(mk((const void*)pc, len)), "s"(((b >> 3) * 4 + w) * 384) : "memory");
    helper_loads(p, L, rw, b, w4, lane);
  }
  if (tid < 8) *(LAS unsigned*)(L + L_FLAG + lane * 4) = 0u;
  const int isW = (NF - 1 - b) >> 31, gF = (b * 1171) >> 16, jW = b - NF;
  const int grp = (gF & ~isW) | ((jW >> 3) & isW), prod = ((b - gF * 56) & ~isW) | ((56 + (jW & 7)) & isW);
  Attn A;
  A.rk = mk(p.k, 64 << 20); A.rv = mk(p.v, 64 << 20);
  A.base = grp * (16 << 20) + (((prod * 4 * NTF + w4 * (WMAP ? NTF : 1)) & ~isW) | ((56 * 4 * NTF + (prod - 56) * 4 + w4) & isW)) * 8192;
  A.ring = (unsigned)(size_t)L + w4 * 32768;
#pragma unroll
  for (int j = 0; j < 4; ++j) A.vk[j] = (lane >> 4) * 256 + (((lane & 15) ^ (4 * (lane >> 4) + j)) << 4);
  A.vn = lane * 16; A.L = L; A.lane = lane; A.w = w4;
  SB();
  barrier();
  if (r1 != 0) { issueK1<0>(A, 0); issueK1<1>(A, 0); issueK1<2>(A, 0); issueK1<3>(A, 0); }
  STAMP(11);
  unsigned tag;
  if (r2 == 0) helper_wave(L, rw, b, w4, lane, grp, t0, tag STAMP_PASS);
  v4u G[8];
  int b0 = b;
  asm volatile("" : "+s"(b0));
  if (b0 < NF) {
    const int cg = QM ? (b & 7) * 7 + (b >> 5) : b >> 2, kq = QM ? (b >> 3) & 3 : b & 3;
    const int gvo = (4 * ((lane & 15) >> 3) + (lane >> 4)) * 7168 + (lane & 7) * 16, gso = (kq * 256 + w4 * 64) * 7168 + cg * 128;
    if (r3 != 0) {
      attn_wave<NTF>(A, G, mk(p.wg, (ABL & 2) ? 0 : 7 << 20), gvo, gso, tag STAMP_PASS);
      ffn_tail(p, L, rw, cg, kq, w4, lane, grp, prod, mktag(tag), G, A.t0 STAMP_PASS);
    }
    if (w == 0) ml_duty(L, rw, lane, WS_ML + grp * 8192 + prod * 16, tag);
  }
  if (b >= NF) {
    if (r3 != 0) attn_wave<1>(A, G, rw, 0, 0, tag STAMP_PASS);
    if (w == 0) ml_duty(L, rw, lane, WS_ML + grp * 8192 + prod * 16, tag);
    barrier();
    const Tag T = mktag(tag);
    if (w >= 4) publish_o(L, rw, tid - 256, grp, prod, T);
    STAMP(5);
    wo_tail(p, L, rw, jW, w, lane, T STAMP_PASS);
    asm volatile(".Lcode_to:");
  }
  if ((b >= NF) == (AMPW != 0) && (b >= NF || w >= 4) && stamp[AMP] != 0) {
    const int tb0 = __builtin_amdgcn_readfirstlane(*(const LAS unsigned*)(L + L_TAG + 4));
    int d = (int)stamp[AMP] - tb0; d = AMPS * d; if (d > 5000) d = 5000; if (d < -250) d = -250;
    while ((int)rtc() - tb0 < 10000 + 20 * d) __builtin_amdgcn_s_sleep(1);
  }
}

extern "C" void kernel_launch(void* const* d_in, const int* in_sizes, int n_in, void* d_out, int out_size, void* d_ws, size_t ws_size, hipStream_t stream) {
  Params p;
  p.x = d_in[0]; p.k = d_in[1]; p.v = d_in[2]; p.wq = d_in[3]; p.wo = d_in[4]; p.wg = d_in[6]; p.wu = d_in[7]; p.wd = d_in[8];
  p.out = d_out; p.ws = d_ws;
  decode_block_kernel<<<256, 512, 0, stream>>>(p);
}
```

```cpp
#include <hip/hip_runtime.h>
#include <stdint.h>

#define PD 8
#define WD 4
#define N1 60
#define N2 136
#define NP0 8
#define QSLEEP 8
#define GEARLY 4
#define HSLEEP 0
#define STAG 1
#define WMAP 1
#define CODEPF 1
#define ABL 0
#define NML 132
#define NPRE 0
#define DFIRST 0
#define DLATE 4
#define DWHEN 2
#define GE2 1
#define TUNE 0
#define STAMPS 0
#define KNOBS(X) X(TOFF, 190) X(TAUA, 208) X(TAU, 235) X(TAUC, 208) X(TAU2, 225) X(TW0, 950) X(TWS, 30) X(QDELAY, 750) X(TD0, 430) X(QD1, 470) X(TOP, 160) X(TS0, 0) X(D2, 190) X(D3, 325) X(D4, 440) X(RS2, 45)
struct Kn {
#define X(n, v) static constexpr int n = v;
  KNOBS(X)
#undef X
};

#define DI __device__ __forceinline__
#define LAS __attribute__((address_space(3)))
typedef unsigned v4u __attribute__((ext_vector_type(4)));
typedef unsigned v2u __attribute__((ext_vector_type(2)));
typedef float f32x4 __attribute__((ext_vector_type(4)));
typedef float f32x2 __attribute__((ext_vector_type(2)));
typedef __bf16 bf16x2 __attribute__((ext_vector_type(2)));
typedef __bf16 bf16x8 __attribute__((ext_vector_type(8)));
typedef __bf16 bf16x16 __attribute__((ext_vector_type(16)));
typedef unsigned v8u __attribute__((ext_vector_type(8)));
typedef unsigned long long u64;
typedef __amdgpu_buffer_rsrc_t rsrc_t;

constexpr int NF = 224;
constexpr int NTF = 9;
constexpr float SCALE = 0.08838834764831845f, LOG2E = 1.4426950408889634f, EPS = 1e-5f;
constexpr int AUX_NT = 2, AUX_SC1 = 16;
constexpr int SPIN_CAP = 1 << 14;

constexpr int WS_EPOCH = 0, WS_T0 = 512, WS_HINT = 1024, WS_QP = 4096, WS_ML = WS_QP + 16384, WS_HP = WS_ML + 32768, WS_GU = WS_HP + 131072,
              WS_ST = WS_GU + 56 * 16 * 1024, WS_DP = WS_ST + NF * 128, WS_OP = WS_DP + 32 * 112 * 128, WS_QQ = WS_OP + (1 << 20), WS_END = WS_QQ + (1 << 20);
constexpr int WS_STAMP = 4 << 20;
static_assert(WS_END <= WS_STAMP, "");
constexpr int L_RING = 0, L_Q = 131072, L_ML = L_Q + 2048, L_F = L_ML + 256, L_FLAG = L_F + 128, L_QFLAG = L_FLAG + 16, L_TAG = L_QFLAG + 16, L_RED = L_TAG + 16,
              L_ST = L_RED + 4096, L_SINK = L_ST + 128, L_RED2 = L_SINK + 256, L_X = L_RED2 + 4096, L_END = L_X + 2048;
constexpr int TSTR = WMAP ? 8192 : 32768;
constexpr int OSTR = 528;

struct Params {
  const void* x; const void* k; const void* v; const void* wq; const void* wo; const void* wg; const void* wu; const void* wd;
  void* out; void* ws;
};

DI rsrc_t mk(const void* p, int bytes) { return __builtin_amdgcn_make_buffer_rsrc((void*)p, 0, bytes, 0x00020000); }
DI float asf(unsigned u) { return __builtin_bit_cast(float, u); }
DI unsigned asu(float f) { return __builtin_bit_cast(unsigned, f); }
DI unsigned cvtpk(float a, float b) { f32x2 v = {a, b}; return __builtin_bit_cast(unsigned, __builtin_convertvector(v, bf16x2)); }
DI float bflo(unsigned u) { return asf(u << 16); }
DI float bfhi(unsigned u) { return asf(u & 0xffff0000u); }
DI float rbf(float a) { return bflo(cvtpk(a, a)); }
DI f32x4 mfma(v4u a, v4u b, f32x4 c) { return __builtin_amdgcn_mfma_f32_16x16x32_bf16(__builtin_bit_cast(bf16x8, a), __builtin_bit_cast(bf16x8, b), c, 0, 0, 0); }
DI f32x4 smfma(v4u a, v8u b, f32x4 c, int idx) { return __builtin_amdgcn_smfmac_f32_16x16x64_bf16(__builtin_bit_cast(bf16x8, a), __builtin_bit_cast(bf16x16, b), c, idx, 0, 0); }
template <int CTRL> DI float dppf(float x) { return asf(__builtin_amdgcn_update_dpp(0u, asu(x), CTRL, 0xf, 0xf, true)); }
constexpr int ROR8 = 0x128, ROR4 = 0x124, XOR1 = 0xB1, XOR2 = 0x4E;
DI float add16(float a, float b) { auto s = __builtin_amdgcn_permlane16_swap(asu(a), asu(b), false, false); return asf(s[0]) + asf(s[1]); }
DI float add32(float a, float b) { auto s = __builtin_amdgcn_permlane32_swap(asu(a), asu(b), false, false); return asf(s[0]) + asf(s[1]); }
DI float max16(float a) { auto s = __builtin_amdgcn_permlane16_swap(asu(a), asu(a), false, false); return fmaxf(asf(s[0]), asf(s[1])); }
DI float max32(float a) { auto s = __builtin_amdgcn_permlane32_swap(asu(a), asu(a), false, false); return fmaxf(asf(s[0]), asf(s[1])); }
DI float wave_sum(float a) { a += dppf<XOR1>(a); a += dppf<XOR2>(a); a += dppf<ROR8>(a); a += dppf<ROR4>(a); a = add16(a, a); return add32(a, a); }
DI float wave_max(float a) { a = fmaxf(a, dppf<XOR1>(a)); a = fmaxf(a, dppf<XOR2>(a)); a = fmaxf(a, dppf<ROR8>(a)); a = fmaxf(a, dppf<ROR4>(a)); return max32(max16(a)); }
DI float rs4(f32x4 v) { return add16(add32(v.x, v.z), add32(v.y, v.w)); }
DI u64 rtc() { return __builtin_amdgcn_s_memrealtime(); }
#define SB() __builtin_amdgcn_sched_barrier(0)
#define PINV(x) asm volatile("" : "+v"(x))
#define PINS(x) asm volatile("" : "+s"(x))
#define CFENCE() asm volatile("" ::: "memory")
template <int N> DI void wait_vm() { asm volatile("s_waitcnt vmcnt(%0)" ::"i"(N) : "memory"); }
DI void wait_lds() { asm volatile("s_waitcnt lgkmcnt(0)" ::: "memory"); }
DI void barrier() { asm volatile("s_waitcnt lgkmcnt(0)\n\ts_barrier" ::: "memory"); SB(); }

struct Tag { unsigned t, b0, b1, b2, b3, hi; };
DI Tag mktag(unsigned t) {
  Tag g{t, t & 255u, (t >> 8) & 255u, (t >> 16) & 255u, t >> 24, ~255u};
  PINS(g.b0); PINS(g.b1); PINS(g.b2); PINS(g.b3); PINV(g.hi);
  return g;
}
DI v4u enc(f32x4 v, const Tag& g) { return v4u{(asu(v.x) & g.hi) | g.b0, (asu(v.y) & g.hi) | g.b1, (asu(v.z) & g.hi) | g.b2, (asu(v.w) & g.hi) | g.b3}; }
DI f32x4 dec(v4u v, const Tag& g, unsigned& bad) {
  v.x ^= g.b0; v.y ^= g.b1; v.z ^= g.b2; v.w ^= g.b3;
  bad |= (v.x | v.y) | (v.z | v.w);
  return __builtin_bit_cast(f32x4, v);
}
DI bool all_ok(unsigned bad) { return __builtin_amdgcn_ballot_w64((bad & 255u) != 0u) == 0ull; }
DI v4u ld_sc1(rsrc_t r, int vo, int so) { return __builtin_amdgcn_raw_buffer_load_b128(r, vo, so, AUX_SC1); }
DI void st_sc1(v4u v, rsrc_t r, int vo, int so) { asm volatile("buffer_store_dwordx4 %0, %1, %2, %3 offen sc1\n\ts_nop 1" ::"v"(v), "v"(vo), "s"(r), "s"(so) : "memory"); }
DI v4u ld_nt(rsrc_t r, int vo, int so) { return __builtin_amdgcn_raw_buffer_load_b128(r, vo, so, AUX_NT); }
template <int N, int SLEEP = 0> DI int gather(rsrc_t r, int vo, int so, int stride, const Tag& g, f32x4 (&o)[N], int cap) {
  int spin = 0;
#pragma unroll 1
  for (; spin < cap; ++spin) {
    CFENCE();
    v4u v[N];
#pragma unroll
    for (int i = 0; i < N; ++i) v[i] = ld_sc1(r, vo, so + i * stride);
    unsigned bad = 0;
#pragma unroll
    for (int i = 0; i < N; ++i) o[i] = dec(v[i], g, bad);
    if (all_ok(bad)) break;
    if (SLEEP) __builtin_amdgcn_s_sleep(SLEEP);
  }
  return spin;
}

DI v4u tr1(const v4u (&a)[8], int e) {
  const unsigned sel = (e & 1) ? 0x07060302u : 0x05040100u;
  return v4u{__builtin_amdgcn_perm(a[1][e >> 1], a[0][e >> 1], sel), __builtin_amdgcn_perm(a[3][e >> 1], a[2][e >> 1], sel),
             __builtin_amdgcn_perm(a[5][e >> 1], a[4][e >> 1], sel), __builtin_amdgcn_perm(a[7][e >> 1], a[6][e >> 1], sel)};
}
DI v8u zip(v4u a, v4u b) { v8u r = {a.x, b.x, a.y, b.y, a.z, b.z, a.w, b.w}; asm volatile("" : "+v"(r)); return r; }

#define STAMPX(i)
#define STAMP(i)
#define FAIL(m) do {} while (0)
#define STAMP_ARG
#define STAMP_PASS

struct Attn {
  rsrc_t rk, rv;
  int base;
  unsigned ring;
  int vk[4], vn;
  LAS unsigned char* L;
  int lane, w;
  unsigned t0, lag;
  Kn k;
};
static_assert(PD >= 2 && PD <= 9, "a K tile is read two units after its issue");
template <int J> DI void issueK1(const Attn& A, int t) {
  asm volatile("s_mov_b32 m0, %0\n\ts_nop 0\n\tbuffer_load_dwordx4 %1, %2, %3 offen offset:%4 nt lds"
               ::"s"(A.ring + (t & 3) * 8192 + (J >> 2) * 4096), "v"(A.vk[J & 3]), "s"(A.rk), "s"(A.base + t * TSTR + (J >> 2) * 4096), "i"((J & 3) * 1024) : "memory");
}
DI void clock_wait(unsigned t) {
  asm volatile("1:\n\ts_memrealtime vcc\n\ts_waitcnt lgkmcnt(0)\n\ts_sub_u32 vcc_lo, vcc_lo, %0\n\ts_cmpk_lt_u32 vcc_lo, 0x1000\n\ts_cbranch_scc1 1b"
               ::"s"(t - 0x1000u) : "memory", "scc", "vcc");
}
DI int slot(const Kn& k, int n) { return k.TOFF + (((n < N1 ? n : N1) * k.TAUA + ((n < N1 ? N1 : n > N2 ? N2 : n) - N1) * k.TAU + (n > N2 ? n - N2 : 0) * k.TAUC) >> 4); }
DI int time1(const Kn& k) { return slot(k, 144 + NPRE) + k.TOP; }
template <int N> DI void clock_pace(const Attn& A) { clock_wait(A.t0 + slot(A.k, N)); }
template <int J> struct Ic { static constexpr int v = J; };
template <int U, int J0 = 0, class FI, class FC> DI void unit(Attn& A, FI issue, FC comp) {
#define STEP(J) if (J >= J0) { if (U * 8 + J >= NP0 && (J & 3) == 0) clock_pace<U * 8 + J>(A); wait_vm<PD - 1>(); issue(Ic<J>{}); SB(); comp(Ic<J>{}); SB(); }
  STEP(0) STEP(1) STEP(2) STEP(3) STEP(4) STEP(5) STEP(6) STEP(7)
#undef STEP
}
DI void scoreK(const Attn& A, int t, int u, const v4u (&q)[4], const int (&ra)[4], unsigned (&S)[4], float& mx) {
  f32x4 c = {0.f, 0.f, 0.f, 0.f};
#pragma unroll
  for (int s = 0; s < 4; ++s) c = mfma(*(const LAS v4u*)(A.L + ra[s] + (t & 3) * 8192 + u * 4096), q[s], c);
  mx = fmaxf(fmaxf(mx, c.x), fmaxf(c.y, fmaxf(c.z, c.w)));
  const unsigned a = cvtpk(c.x, c.y), b = cvtpk(c.z, c.w);
  S[2 * u] = cvtpk(bflo(a) * SCALE, bfhi(a) * SCALE);
  S[2 * u + 1] = cvtpk(bflo(b) * SCALE, bfhi(b) * SCALE);
}
DI void expP(unsigned (&S)[4], float m2, float& l) {
#pragma unroll
  for (int i = 0; i < 4; ++i) {
    const float a = __builtin_amdgcn_exp2f(fmaf(bflo(S[i]), LOG2E, -m2)), b = __builtin_amdgcn_exp2f(fmaf(bfhi(S[i]), LOG2E, -m2));
    l += a + b;
    S[i] = cvtpk(a, b);
  }
}
DI void pv(const v4u (&s)[8], const unsigned (&P)[4], f32x4 (&acc)[8], int h) {
  const v4u p = {P[0], P[1], P[2], P[3]};
#pragma unroll
  for (int e = 4 * h; e < 4 * h + 4; ++e) acc[e] = mfma(tr1(s, e), p, acc[e]);
}
DI void wait_flag(LAS unsigned char* cnt) {
  unsigned t;
  asm volatile("s_mov_b32 vcc_hi, 0xfffff\n1:\n\tds_read_b32 %0, %1\n\ts_waitcnt lgkmcnt(0)\n\tv_readfirstlane_b32 vcc_lo, %0\n\ts_sub_u32 vcc_hi, vcc_hi, 1\n\ts_cbranch_scc1 2f\n\t"
               "s_cmp_eq_u32 vcc_lo, 1\n\ts_cbranch_scc0 1b\n2:" : "=&v"(t) : "v"((unsigned)(size_t)cnt) : "memory", "scc", "vcc");
}
DI void put_ml(const Attn& A, float m2, float l) {
  l = add32(add16(l, l), add16(l, l));
  if (A.lane < 8) *(LAS f32x2*)(A.L + L_ML + (A.w * 8 + A.lane) * 8) = f32x2{m2, l};
  wait_lds();
  if (A.lane == 0) *(volatile LAS unsigned*)(A.L + L_FLAG + A.w * 4) = 1u;
}
DI void ml_duty(LAS unsigned char* L, rsrc_t rw, int lane, int mlo, unsigned tag) {
#pragma unroll 1
  for (int spin = 0; spin < SPIN_CAP; ++spin) {
    const v4u f = *(volatile LAS v4u*)(L + L_FLAG);
    if (f.x & f.y & f.z & f.w) break;
    __builtin_amdgcn_s_sleep(8);
  }
  CFENCE();
  const f32x2 ml = *(const LAS f32x2*)(L + L_ML + (lane & 31) * 8);
  const float M = max16(fmaxf(ml.x, dppf<ROR8>(ml.x)));
  const float f = __builtin_amdgcn_exp2f(ml.x - M);
  float s = f * ml.y;
  s += dppf<ROR8>(s);
  s = add16(s, s);
  if (lane < 32) *(LAS float*)(L + L_F + (lane & 7) * 16 + (lane >> 3) * 4) = f;
  if (lane < 8) st_sc1(v4u{asu(M), tag, asu(s), tag}, rw, lane * 16, mlo);
  wait_lds();
}

template <int NT, class PRE0, class PRE> DI void attn_wave(Attn& A, PRE0 pre0, PRE pre, unsigned& tag STAMP_ARG) {
  const int g = A.lane >> 4, i = A.lane & 15;
  v4u V0[8], V1[8], V2[8], V3[8];
  unsigned S[NT][4];
  auto none = [](auto) {};
#define IK(t) [&](auto j) { issueK1<decltype(j)::v>(A, t); }
#define IV(t, R) [&](auto j) { constexpr int J = decltype(j)::v; R[J] = ld_nt(A.rv, A.vn + (J & 3) * 1024, A.base + (t) * TSTR + (J >> 2) * 4096); }
#define CO(...) [&](auto j) { constexpr int J = decltype(j)::v; __VA_ARGS__ }
  v4u q[4]; int ra[4];
  float mx = -INFINITY, l = 0.f, m2;
  f32x4 acc[8];
#define SK(t, u) scoreK(A, t, u, q, ra, S[(t) % NT], mx)
#define EP(t) expP(S[(t) % NT], m2, l)
#define PV(t, R, h) pv(R, S[(t) % NT], acc, h)
  auto getq = [&]() {
    STAMP(1);
    wait_flag(A.L + L_QFLAG + 4);
    STAMP(2);
#pragma unroll
    for (int s = 0; s < 4; ++s) {
      q[s] = *(const LAS v4u*)(A.L + L_Q + (i & 7) * 256 + s * 64 + g * 16);
      ra[s] = A.w * 32768 + (4 * (i & 3) + (i >> 2)) * 256 + (((4 * s + g) ^ i) << 4);
    }
  };
  auto gett = [&]() {
    wait_flag(A.L + L_QFLAG);
    const v2u tt = *(const LAS v2u*)(A.L + L_TAG);
    tag = __builtin_amdgcn_readfirstlane(tt.x); A.t0 = __builtin_amdgcn_readfirstlane(tt.y) + (STAG ? A.w * (A.k.TAU >> 4) : 0);
  };
  auto rowmax = [&]() { return rbf(rbf(max32(max16(mx))) * SCALE) * LOG2E; };
#pragma unroll
  for (int e = 0; e < 8; ++e) acc[e] = f32x4{0.f, 0.f, 0.f, 0.f};
  if (NT == 1) {
    unit<-100, 4>(A, IK(0), none);
    unit<-100>(A, IV(0, V0), none);
    pre(0u);
    getq();
    wait_vm<8>();
    SK(0, 0); SK(0, 1);
    m2 = rowmax();
    EP(0);
    put_ml(A, m2, l);
  } else {
    unit<0, 4>(A, IK(0), none);
    unit<1>(A, IK(1), none);
    unit<2>(A, IK(2), none);
    unit<3>(A, IK(3), none);
    unit<4>(A, IV(0, V0), CO(if (J == 7) gett();));
    unit<5>(A, IV(1, V1), none);
    unit<6>(A, IV(2, V2), none);
    unit<7>(A, IV(3, V3), CO(if (J == 7) { getq(); SK(0, 0); }));
    unit<8>(A, IK(4), CO(if (J < 7) SK((J + 1) >> 1, (J + 1) & 1); else { m2 = rowmax(); EP(0); }));
    unit<9>(A, IK(5), CO(if (J < 2) PV(0, V0, J); if (J == 2) EP(1); if (J == 3 || J == 4) PV(1, V1, J - 3); if (J == 5) EP(2); if (J >= 6) PV(2, V2, J - 6);));
    unit<10>(A, IK(6), CO(if (J < 2) SK(4, J);));
    unit<11>(A, IK(7), CO(if (J < 2) SK(5, J);));
    unit<12>(A, IK(8), CO(if (J < 2) SK(6, J);));
    unit<13>(A, IV(4, V0), CO(if (J < 2) SK(7, J);));
    unit<14>(A, IV(5, V1), CO(if (J < 2) SK(8, J);
                              if (J == 2) {
                                const float mn = rowmax(), al = __builtin_amdgcn_exp2f(m2 - mn);
                                m2 = mn; l *= al;
                                _Pragma("unroll") for (int e = 0; e < 8; ++e) acc[e] *= al;
                              }
                              if (J >= 3) EP(J);));
    STAMP(3);
    unit<15>(A, IV(6, V2), CO(if (J == 0) EP(8); if (J == 1) put_ml(A, m2, l); if (J == 2 || J == 3) PV(3, V3, J - 2); if (J == 4 || J == 5) PV(4, V0, J - 4);));
    unit<16>(A, IV(7, V3), CO(if (J < 2) PV(5, V1, J);));
    pre0();
    A.t0 += (NPRE * A.k.TAUC) >> 4;
    unit<17>(A, IV(8, V0), CO(if (J < 2) PV(6, V2, J);));
    PV(7, V3, 0); PV(7, V3, 1);
    SB();
    pre(tag);
  }
  LAS unsigned char* img = A.L + A.w * 32768 + (i & 7) * OSTR + g * 128;
  PINV(img);
  v4u F[8];
  const v4u pl = {S[NT - 1][0], S[NT - 1][1], S[NT - 1][2], S[NT - 1][3]};
#pragma unroll
  for (int e = 0; e < 8; ++e) {
    const unsigned sel = (e & 1) ? 0x07060302u : 0x05040100u;
    F[e].x = __builtin_amdgcn_perm(V0[1][e >> 1], V0[0][e >> 1], sel); F[e].y = __builtin_amdgcn_perm(V0[3][e >> 1], V0[2][e >> 1], sel);
  }
  SB();
#pragma unroll
  for (int e = 0; e < 8; ++e) {
    const unsigned sel = (e & 1) ? 0x07060302u : 0x05040100u;
    F[e].z = __builtin_amdgcn_perm(V0[5][e >> 1], V0[4][e >> 1], sel); F[e].w = __builtin_amdgcn_perm(V0[7][e >> 1], V0[6][e >> 1], sel);
    if (e >= 1) acc[e - 1] = mfma(F[e - 1], pl, acc[e - 1]);
    if (e >= 4) *(LAS f32x4*)(img + (e - 4) * 16) = acc[e - 4];
    SB();
  }
  acc[7] = mfma(F[7], pl, acc[7]);
#pragma unroll
  for (int e = 4; e < 8; ++e) *(LAS f32x4*)(img + e * 16) = acc[e];
  STAMP(4);
}
struct OPub { LAS unsigned char *a, *b, *f; int vo, so; };
DI OPub prep_o(LAS unsigned char* L, int tid, int grp, int prod) {
  const int h = tid >> 5, c = tid & 31;
  OPub o{L + h * OSTR + c * 16, L + 65536 + h * OSTR + c * 16, L + L_F + h * 16, h * 32768 + c * 16, WS_OP + grp * 8 * 32768 + prod * 512};
  PINV(o.a); PINV(o.b); PINV(o.f); PINV(o.vo); PINS(o.so);
  return o;
}
DI void publish_o(const OPub& o, rsrc_t rw, const Tag& T) {
  const f32x4 f = *(const LAS f32x4*)o.f;
  const f32x4 a0 = *(const LAS f32x4*)o.a, a1 = *(const LAS f32x4*)(o.a + 32768), a2 = *(const LAS f32x4*)o.b, a3 = *(const LAS f32x4*)(o.b + 32768);
  st_sc1(enc((f.x * a0 + f.y * a1) + (f.z * a2 + f.w * a3), T), rw, o.vo, o.so);
}

DI unsigned next_tag(unsigned e) { return ((e & 0x7f7f7f7fu) + 0x01010101u) ^ (e & 0x80808080u); }
DI void helper_wave(LAS unsigned char* L, rsrc_t rw, int lane, int grp, unsigned tag, unsigned town, const Kn& k STAMP_ARG) {
  v2u tz = {town, tag};
  clock_wait(town + k.TD0);
  if (!(ABL & 8))
#pragma unroll 1
  for (int spin = 0; spin < SPIN_CAP; ++spin) {
    CFENCE();
    tz = __builtin_amdgcn_raw_buffer_load_b64(rw, (lane & 7) * 8, WS_T0, AUX_SC1);
    if (__builtin_amdgcn_ballot_w64(tz.y != tag) == 0ull) break;
    if (spin > 2000) FAIL(0x100);
    __builtin_amdgcn_s_sleep(4);
  }
  const unsigned tb = __builtin_amdgcn_readfirstlane(tz.x);
  int d = (int)(tz.x - tb);
  d += __builtin_amdgcn_update_dpp(0, d, XOR1, 0xf, 0xf, true); d += __builtin_amdgcn_update_dpp(0, d, XOR2, 0xf, 0xf, true); d += __builtin_amdgcn_update_dpp(0, d, ROR4, 0xf, 0xf, true);
  const unsigned t0 = tb + (unsigned)(__builtin_amdgcn_readfirstlane(d) >> 3);
  if (lane == 0) *(LAS v2u*)(L + L_TAG) = v2u{tag, t0};
  wait_lds();
  if (lane == 0) *(volatile LAS unsigned*)(L + L_QFLAG) = 1u;
  STAMP(1);
  clock_wait(t0 + k.QDELAY);
  v4u v[4];
#pragma unroll 1
  for (int spin = 0; spin < SPIN_CAP; ++spin) {
    CFENCE();
#pragma unroll
    for (int i = 0; i < 4; ++i) v[i] = ld_sc1(rw, lane * 16 + i * 1024, WS_QP + grp * 4096);
    unsigned bad = 0;
#pragma unroll
    for (int i = 0; i < 4; ++i) bad |= (v[i].y ^ tag) | (v[i].w ^ tag);
    if (__builtin_amdgcn_ballot_w64(bad != 0u) == 0ull) break;
    if (spin > 2000) FAIL(0x200);
    __builtin_amdgcn_s_sleep(QSLEEP);
  }
#pragma unroll
  for (int i = 0; i < 4; ++i) *(LAS v2u*)(L + L_Q + i * 512 + lane * 8) = v2u{v[i].x, v[i].z};
  wait_lds();
  if (lane == 0) *(volatile LAS unsigned*)(L + L_QFLAG + 4) = 1u;
  STAMP(2);
}
DI void q_part(const Params& p, LAS unsigned char* L, rsrc_t rw, int b, int w, int lane, unsigned t0, unsigned& tag STAMP_ARG) {
  const int g = lane >> 4, n = lane & 15, ks = b >> 3, head = (b & 7) * 4 + w;
  const rsrc_t rx = mk(p.x, 2048), rq = mk(p.wq, (ABL & 16) ? 0 : 8 << 20);
  const unsigned e0 = __builtin_amdgcn_raw_buffer_load_b32(rw, 0, WS_EPOCH, AUX_SC1);
  const v4u xa = __builtin_amdgcn_raw_buffer_load_b128(rx, lane * 16, 0, 0), xb = __builtin_amdgcn_raw_buffer_load_b128(rx, lane * 16, 1024, 0);
  const v4u xo = __builtin_amdgcn_raw_buffer_load_b128(rx, g * 16, ks * 64, 0);
  const int vo = (8 * (g >> 1) + 4 * (g & 1)) * 8192 + n * 16, so = ks * 32 * 8192 + head * 256, idx = (n & 4) ? 0xEEEE : 0x4444;
  v4u W[8];
#pragma unroll
  for (int j = 0; j < 8; ++j) W[j] = ld_nt(rq, vo, so + (16 * (j >> 2) + (j & 3)) * 8192);
  SB();
  tag = next_tag(__builtin_amdgcn_readfirstlane(e0));
  if (b < 8 && w == 3 && lane == 0) __builtin_amdgcn_raw_buffer_store_b64(v2u{t0, tag}, rw, 0, WS_T0 + b * 8, AUX_SC1);
  if (w == 0) { *(LAS v4u*)(L + L_X + lane * 16) = xa; *(LAS v4u*)(L + L_X + 1024 + lane * 16) = xb; }
  float s = 0.f;
#pragma unroll
  for (int k = 0; k < 4; ++k) s += (bflo(xa[k]) + bfhi(xa[k])) + (bflo(xb[k]) + bfhi(xb[k]));
  const float mean = wave_sum(s) * (1.f / 1024.f);
  float v = 0.f;
#pragma unroll
  for (int k = 0; k < 4; ++k) {
    const float d0 = bflo(xa[k]) - mean, d1 = bfhi(xa[k]) - mean, d2 = bflo(xb[k]) - mean, d3 = bfhi(xb[k]) - mean;
    v += (d0 * d0 + d1 * d1) + (d2 * d2 + d3 * d3);
  }
  const float var = wave_sum(v) * (1.f / 1024.f);
  const float mb = rbf(mean), sd = rbf(sqrtf(rbf(rbf(var) + EPS)));
  v4u X;
#pragma unroll
  for (int k = 0; k < 4; ++k) X[k] = cvtpk(rbf(bflo(xo[k]) - mb) / sd, rbf(bfhi(xo[k]) - mb) / sd);
  SB();
  f32x4 a[4];
#pragma unroll
  for (int e = 0; e < 4; ++e) a[e] = smfma(X, zip(tr1(W, e), tr1(W, e + 4)), f32x4{0.f, 0.f, 0.f, 0.f}, idx);
  const bool hi = g >> 1;
  st_sc1(v4u{asu(hi ? a[2].x : a[0].x), tag, asu(hi ? a[3].x : a[1].x), tag}, rw, n * 64 + (g & 1) * 32 + (g >> 1) * 16, WS_QQ + (head * 32 + ks) * 1024);
  STAMP(3);
}
DI void q_sum(LAS unsigned char* L, rsrc_t rw, int j, int w, int lane, unsigned t0, unsigned tag STAMP_ARG) {
  clock_wait(t0);
  v4u v[4];
#pragma unroll 1
  for (int spin = 0; spin < SPIN_CAP; ++spin) {
    CFENCE();
#pragma unroll
    for (int i = 0; i < 4; ++i) v[i] = ld_sc1(rw, lane * 16 + i * 1024, WS_QQ + j * 32768 + w * 4096);
    unsigned bad = 0;
#pragma unroll
    for (int i = 0; i < 4; ++i) bad |= (v[i].y ^ tag) | (v[i].w ^ tag);
    if (__builtin_amdgcn_ballot_w64(bad != 0u) == 0ull) break;
    if (spin > 2000) FAIL(0x400);
    __builtin_amdgcn_s_sleep(4);
  }
  *(LAS f32x2*)(L + L_RED + w * 512 + lane * 8) = f32x2{(asf(v[0].x) + asf(v[1].x)) + (asf(v[2].x) + asf(v[3].x)), (asf(v[0].z) + asf(v[1].z)) + (asf(v[2].z) + asf(v[3].z))};
  barrier();
  if (w == 1) {
    const LAS f32x2* r = (const LAS f32x2*)(L + L_RED + lane * 8);
    const f32x2 q2 = ((r[0] + r[64]) + (r[128] + r[192])) + ((r[256] + r[320]) + (r[384] + r[448]));
    __builtin_amdgcn_raw_buffer_store_b64(v2u{cvtpk(q2.x, q2.y), tag}, rw, lane * 8, WS_QP + j * 512, AUX_SC1);
  }
  STAMP(11);
}

DI void ffn_wave(const Params& p, Attn& A, rsrc_t rw, int b, int grp, int prod, unsigned& tag STAMP_ARG) {
  LAS unsigned char* L = A.L;
  const Kn& k = A.k;
  const int w = A.w, lane = A.lane, g = lane >> 4, n = lane & 15, cq = b >> 4, ks = b & 15, c32 = cq * 8 + (b & 7), oh = (b >> 3) & 1;
  const int wbytes = (ABL & 2) ? 0 : 7 << 20;
  const rsrc_t rm = mk((w & 1) ? p.wu : p.wg, wbytes), rd = mk(p.wd, wbytes);
#define RU(j) (4 * ((j) & 3) + 2 * (((j) >> 2) & 1) + 32 * ((j) >> 3))
  const int rl = 16 * (g & 1) + (g >> 1), vo = rl * 7168 + n * 16, so = ks * 64 * 7168 + cq * 512 + (w >> 1) * 256;
  v4u G[8], U[8], Dn[8];
  Tag T; OPub op;
  int dvo = rl * 2048 + n * 16, dso = c32 * 32 * 2048 + oh * 1024 + w * 256;
#define LD_D(j) Dn[j] = ld_nt(rd, dvo, dso + RU(j) * 2048)
  auto ldw = [&](auto k) {
    constexpr int K = decltype(k)::v, J = K - DFIRST;
    if (K < DFIRST) LD_D(K & 7);
    else if (J < 8) G[J & 7] = ld_nt(rm, vo, so + RU(J & 7) * 7168);
    else if (J < 16) U[J & 7] = ld_nt(rm, vo, so + RU(J & 15) * 7168);
    else LD_D((K - 16) & 7);
    SB();
  };
#define LD4(k) { ldw(Ic<(k)>{}); ldw(Ic<(k) + 1>{}); ldw(Ic<(k) + 2>{}); ldw(Ic<(k) + 3>{}); }
#define LD4P(k) { wait_vm<PD - 1>(); ldw(Ic<(k)>{}); wait_vm<PD - 1>(); ldw(Ic<(k) + 1>{}); wait_vm<PD - 1>(); ldw(Ic<(k) + 2>{}); wait_vm<PD - 1>(); ldw(Ic<(k) + 3>{}); }
  attn_wave<NTF>(A, [&]() {
    if (NPRE >= 4) { clock_wait(A.t0 + slot(k, 136)); LD4P(0) }
    if (NPRE >= 8) { clock_wait(A.t0 + slot(k, 140)); LD4P(4) }
    if (NPRE >= 12) { clock_wait(A.t0 + slot(k, 144)); LD4P(8) }
  }, [&](unsigned tg) {
    T = mktag(tg);
    op = prep_o(L, w * 64 + lane, grp, prod);
    SB();
    if (GEARLY) { clock_pace<144>(A); LD4P(NPRE) }
  }, tag STAMP_PASS);
  if (GE2) LD4(NPRE + GEARLY)
  barrier();
  publish_o(op, rw, T);
  STAMP(5);
  constexpr int K0 = NPRE + GEARLY + 4 * GE2, K1 = 24 - DLATE;
  const unsigned ts = A.t0 + slot(k, 144) + k.TS0;
#define GRP(kk) if ((kk) >= K0 && (kk) < K1) { clock_wait(ts + ((((kk) - NPRE) * k.TAU2) >> 4)); LD4(kk) }
  GRP(0) GRP(4) GRP(8)
  PINV(dvo); PINS(dso);
  unsigned t2 = A.t0 - (STAG ? w * (k.TAU >> 4) : 0) - ((NPRE * k.TAUC) >> 4) + time1(k) + k.D2, t3 = t2 - k.D2 + k.D3;
  PINS(t2); PINS(t3);
  int idx = (n & 4) ? 0xEEEE : 0x4444;
  float xo = asf((unsigned)*(const LAS unsigned short*)(L + L_X + ks * 128 + (4 * n + g) * 2) << 16);
  LAS unsigned char *r1w = L + L_RED + lane * 16 + w * 4, *r1r = L + L_RED + lane * 16, *r2w = L + L_RED2 + w * 1024 + lane * 16, *r2r = L + L_RED2 + lane * 16;
  LAS unsigned char *stw = L + L_ST + (w * 4 + (lane & 3)) * 8, *str = L + L_ST + n * 8;
  int hvo = g * 4096 + n * 16, hso = WS_HP + w * 32768 + ks * 256;
  int gvo = (n >> 2) * 8192 + ((w & 1) * 2 + (g >> 1)) * 128 + (n & 3) * 32 + (g & 1) * 16, gso = WS_GU + (cq * 8 + (w >> 1) * 4) * 8192 + ks * 512;
  int cvo = (n >> 3) * 512 + g * 128 + (n & 7) * 16, cso = WS_GU + c32 * 8192 + w * 2048, svo = (lane & 3) * 128, sso = WS_ST + (cq * 16 + w * 4) * 128;
  int ovo = (n >> 2) * (112 * 128) + (n & 3) * 32 + (g & 1) * 16, oso = WS_DP + ((oh * 16 + w * 4) * 112 + c32) * 128;
  PINV(idx); PINV(xo); PINV(r1w); PINV(r1r); PINV(r2w); PINV(r2r); PINV(stw); PINV(str); PINV(hvo); PINS(hso); PINV(gvo); PINS(gso);
  PINV(cvo); PINS(cso); PINV(svo); PINS(sso); PINV(ovo); PINS(oso);
  f32x4 a[4], d[4];
  v4u X0 = {0x3f803f80u, 0x3f803f80u, 0x3f803f80u, 0x3f803f80u}, X1 = X0;
#pragma unroll
  for (int e = 0; e < 4; ++e) { a[e] = f32x4{0.f, 0.f, 0.f, 0.f}; d[e] = a[e]; PINV(a[e]); PINV(d[e]); }
  PINV(X0); PINV(X1);
  SB();
  v8u GG[4], UU[4], DD[4];
  GRP(12)
  if (DFIRST) { GRP(16) }
#pragma unroll
  for (int e = 0; e < 4; ++e) GG[e] = zip(tr1(G, e), tr1(G, e + 4));
  SB();
  if (!DFIRST) { GRP(16) }
  GRP(20)
  STAMP(6);
  clock_wait(t2);
  STAMP(7);
  f32x4 hp[2];
  {
    const v4u v0 = ld_sc1(rw, hvo, hso), v1 = ld_sc1(rw, hvo, hso + 16384);
    SB();
    if (DLATE && DWHEN == 2) { if (DLATE == 8) LD4(16) LD4(20) }
#pragma unroll
    for (int e = 0; e < 4; ++e) UU[e] = zip(tr1(U, e), tr1(U, e + 4));
    SB();
    unsigned bad = 0;
    hp[0] = dec(v0, T, bad); hp[1] = dec(v1, T, bad);
    if (!all_ok(bad)) { gather<2, HSLEEP>(rw, hvo, hso, 16384, T, hp, SPIN_CAP); t3 = (unsigned)rtc() + (k.D3 - k.D2 - k.RS2); FAIL(2); }
  }
  *(LAS float*)r1w = rs4(hp[0] + hp[1]);
  if (DLATE && DWHEN == 0) { if (DLATE == 8) LD4(16) LD4(20) }
  barrier();
  STAMP(8);
  {
    const f32x4 hs = *(const LAS f32x4*)r1r;
    const float h = xo + rbf((hs.x + hs.y) + (hs.z + hs.w));
    const unsigned pk = cvtpk(h, dppf<0x101>(h));
#define BC(o, k) (unsigned)__builtin_amdgcn_update_dpp(o, pk, 0x150 + (k), 0xf, 0x3, false)
    X0 = v4u{BC(X0.x, 0), BC(X0.y, 2), BC(X0.z, 4), BC(X0.w, 6)};
    SB();
    a[0] = smfma(X0, GG[0], a[0], idx); X1.x = BC(X1.x, 8); SB();
    a[1] = smfma(X0, GG[1], a[1], idx); X1.y = BC(X1.y, 10); SB();
    a[2] = smfma(X0, GG[2], a[2], idx); X1.z = BC(X1.z, 12); X1.w = BC(X1.w, 14); SB();
    a[3] = smfma(X0, GG[3], a[3], idx); SB();
#undef BC
#pragma unroll
    for (int e = 0; e < 4; ++e) a[e] = smfma(X1, UU[e], a[e], idx);
    SB();
  }
  f32x4 gram = mfma(X1, X1, mfma(X0, X0, f32x4{0.f, 0.f, 0.f, 0.f}));
  SB();
  st_sc1(enc(f32x4{a[0].x, a[1].x, a[2].x, a[3].x}, T), rw, gvo, gso);
  if (w == 0) {
    if (lane < 8) st_sc1(v4u{(unsigned)__builtin_amdgcn_readlane(asu(gram.x), 8), T.t, (unsigned)__builtin_amdgcn_readlane(asu(gram.x), 0), T.t}, rw, lane * 16, WS_ST + b * 128);
  }
  STAMP(9);
  if (DLATE && DWHEN == 1) { if (DLATE == 8) LD4(16) LD4(20) }
  if (b == 0 && w == 2 && lane == 0) __builtin_amdgcn_raw_buffer_store_b32(T.t, rw, 0, WS_EPOCH, AUX_SC1);
  clock_wait(t3);
  {
    f32x4 gu[2];
    CFENCE();
    v4u v0 = ld_sc1(rw, cvo, cso), v1 = ld_sc1(rw, cvo + 1024, cso), st = ld_sc1(rw, svo, sso);
    SB();
#pragma unroll
    for (int e = 0; e < 4; ++e) DD[e] = zip(tr1(Dn, e), tr1(Dn, e + 4));
    SB();
#pragma unroll 1
    for (int spin = 0; spin < SPIN_CAP; ++spin) {
      unsigned bad = 0;
      gu[0] = dec(v0, T, bad); gu[1] = dec(v1, T, bad);
      if (__builtin_amdgcn_ballot_w64(((bad & 255u) | (st.y ^ T.t) | (st.w ^ T.t)) != 0u) == 0ull) break;
      CFENCE(); FAIL(4);
      v0 = ld_sc1(rw, cvo, cso); v1 = ld_sc1(rw, cvo + 1024, cso); st = ld_sc1(rw, svo, sso);
    }
    *(LAS f32x4*)r2w = gu[0] + gu[1];
    *(LAS f32x2*)stw = f32x2{asf(st.x), asf(st.z)};
  }
  barrier();
  STAMP(10);
  const f32x2 sv = *(const LAS f32x2*)str;
  const LAS f32x4* r = (const LAS f32x4*)r2r;
  const f32x4 t = (r[0] + r[64]) + (r[128] + r[192]);
  float s1 = sv.x, s2 = sv.y;
  s1 += dppf<ROR8>(s1); s2 += dppf<ROR8>(s2); s1 += dppf<ROR4>(s1); s2 += dppf<ROR4>(s2);
  s1 += dppf<XOR2>(s1); s2 += dppf<XOR2>(s2); s1 += dppf<XOR1>(s1); s2 += dppf<XOR1>(s2);
  const float mean = s1 * (1.f / 1024.f), var = fmaxf(fmaf(-mean, mean, s2 * (1.f / 1024.f)), 0.f);
  const float mb = rbf(mean), rinv = __builtin_amdgcn_rcpf(rbf(__builtin_amdgcn_sqrtf(rbf(rbf(var) + EPS))));
  const auto p0 = __builtin_amdgcn_permlane32_swap(asu(t.x), asu(t.z), false, false), p1 = __builtin_amdgcn_permlane32_swap(asu(t.y), asu(t.w), false, false);
  const auto q0 = __builtin_amdgcn_permlane16_swap(p0[0], p1[0], false, false), q1 = __builtin_amdgcn_permlane16_swap(p0[1], p1[1], false, false);
  float lg = fmaf(-mb, asf(q0[1]), asf(q0[0])), lu = fmaf(-mb, asf(q1[1]), asf(q1[0]));
  lg += dppf<ROR8>(lg); lu += dppf<ROR8>(lu);
  const unsigned gub = cvtpk(lg * rinv, lu * rinv);
  const float gv = bflo(gub), uv = bfhi(gub);
  const float y = gv * fmaf(gv * gv, 0.044715f * (-2.f * 0.7978845608028654f * LOG2E), -2.f * 0.7978845608028654f * LOG2E);
  const float act = rbf(gv * __builtin_amdgcn_rcpf(1.f + __builtin_amdgcn_exp2f(y))) * uv;
  const unsigned pk = cvtpk(act, dppf<0x101>(act));
#define BC(k) (unsigned)__builtin_amdgcn_update_dpp(0u, pk, 0x150 + (k), 0xf, 0xf, true)
  const v4u Xa = {BC(0), BC(2), BC(4), BC(6)};
#undef BC
#pragma unroll
  for (int e = 0; e < 4; ++e) d[e] = smfma(Xa, DD[e], d[e], idx);
  if (lane < 32) st_sc1(enc(f32x4{d[0].x, d[1].x, d[2].x, d[3].x}, T), rw, ovo, oso);
  STAMP(11);
}

DI void wo_tail(const Params& p, LAS unsigned char* L, rsrc_t rw, int j, int w, int lane, const Tag& T, const Kn& k STAMP_ARG) {
  const int g = lane >> 4, n = lane & 15;
  const rsrc_t ro = mk(p.wo, (ABL & 1) ? 0 : 8 << 20), rout = mk(p.out, 2048);
  const unsigned tg0 = __builtin_amdgcn_readfirstlane(*(const LAS unsigned*)(L + L_TAG + 4)), t0 = tg0 + w * (k.TWS >> 3);
  v4u W[8];
  v8u WW[4][4];
  const int wvo = (32 * (g >> 1) + (g & 1)) * 2048 + n * 16;
#define LD_W(t, jj) clock_wait(t0 + k.TW0 + ((t) * 8 + (jj)) * k.TWS); wait_vm<WD - 1>(); W[jj] = ld_nt(ro, wvo, (j * 128 + 64 * ((jj) >> 2) + 8 * ((jj) & 3) + 2 * (t)) * 2048 + w * 256); SB();
#define LD_WT(t) LD_W(t, 0) LD_W(t, 1) LD_W(t, 2) LD_W(t, 3) LD_W(t, 4) LD_W(t, 5) LD_W(t, 6) LD_W(t, 7) \
  _Pragma("unroll") for (int e = 0; e < 4; ++e) WW[t][e] = zip(tr1(W, e), tr1(W, e + 4)); SB();
  LD_WT(0) LD_WT(1) LD_WT(2) LD_WT(3)
#undef LD_WT
#undef LD_W
  STAMP(6);
  clock_wait(t0 + slot(k, NML));
  v4u ml;
#pragma unroll 1
  for (int spin = 0; spin < SPIN_CAP; ++spin) {
    CFENCE();
    ml = ld_sc1(rw, lane * 128, WS_ML + (j >> 3) * 8192 + (j & 7) * 16);
    if (__builtin_amdgcn_ballot_w64(((ml.y ^ T.t) | (ml.w ^ T.t)) != 0u) == 0ull) break;
    if (spin > 2000) FAIL(0x800);
    __builtin_amdgcn_s_sleep(16);
  }
  const float wp = __builtin_amdgcn_exp2f(asf(ml.x) - wave_max(asf(ml.x)));
  const float cp = wp / wave_sum(wp * asf(ml.z));
  float c[4];
  f32x4 acc[4];
#pragma unroll
  for (int i = 0; i < 4; ++i) {
    const float a = asf(__builtin_amdgcn_readlane(asu(cp), 8 * w + 2 * i)), bb = asf(__builtin_amdgcn_readlane(asu(cp), 8 * w + 2 * i + 1));
    c[i] = lane < 32 ? a : bb;
    acc[i] = f32x4{0.f, 0.f, 0.f, 0.f};
    PINV(c[i]); PINV(acc[i]);
  }
  int idx = (n & 4) ? 0xEEEE : 0x4444, vl = lane * 16, oso = WS_OP + j * 32768 + w * 4096, hvo = n * 32 + g * 16, hso = WS_HP + j * 4096 + w * 512, dso = WS_DP + j * 112 * 128 + w * 2048;
  LAS unsigned char *rdw = L + L_RED + w * 512 + (lane & 31) * 16 + (lane >> 5) * 8, *rdr = L + L_RED + lane * 8, *fw = L + L_RED2 + lane * 32 + w * 4, *fr = L + L_RED2 + lane * 32;
  float xo = asf((unsigned)*(const LAS unsigned short*)(L + L_X + j * 64 + (4 * (lane & 7) + g) * 2) << 16);
  int ovo = (4 * (lane & 7) + g) * 2;
  PINV(idx); PINV(vl); PINS(oso); PINV(hvo); PINS(hso); PINS(dso); PINV(rdw); PINV(rdr); PINV(fw); PINV(fr); PINV(xo); PINV(ovo);
  SB();
  unsigned t1 = tg0 + time1(k), t4 = t1 + (w == 7 ? k.D2 + 60 : k.D4);
  PINS(t4);
  clock_wait(t1);
  STAMP(7);
  f32x4 op[4];
  { const int sp_ = gather<4>(rw, vl, oso, 1024, T, op, SPIN_CAP); if (sp_) FAIL(1);
  }
  const f32x4 o4 = (c[0] * op[0] + c[1] * op[1]) + (c[2] * op[2] + c[3] * op[3]);
  *(LAS f32x2*)rdw = f32x2{add32(o4.x, o4.z), add32(o4.y, o4.w)};
  barrier();
  STAMP(8);
  const LAS f32x2* r = (const LAS f32x2*)rdr;
  const f32x2 o2 = ((r[0] + r[64]) + (r[128] + r[192])) + ((r[256] + r[320]) + (r[384] + r[448]));
  const unsigned ob = cvtpk(o2.x, o2.y);
#define BC(k) (unsigned)__builtin_amdgcn_update_dpp(0u, ob, 0x150 + (k), 0xf, 0xf, true)
  v4u X = {BC(0), BC(1), BC(2), BC(3)}, Y;
  SB();
#define STEP(t, XA, XB, k) acc[0] = smfma(XA, WW[t][0], acc[0], idx); XB.x = BC(k); SB(); acc[1] = smfma(XA, WW[t][1], acc[1], idx); XB.y = BC(k + 1); SB(); \
  acc[2] = smfma(XA, WW[t][2], acc[2], idx); XB.z = BC(k + 2); XB.w = BC(k + 3); SB(); acc[3] = smfma(XA, WW[t][3], acc[3], idx); SB();
  STEP(0, X, Y, 4) STEP(1, Y, X, 8) STEP(2, X, Y, 12)
#undef STEP
#undef BC
#pragma unroll
  for (int e = 0; e < 4; ++e) acc[e] = smfma(Y, WW[3][e], acc[e], idx);
  if (lane < 32) st_sc1(enc(f32x4{acc[0].x, acc[1].x, acc[2].x, acc[3].x}, T), rw, hvo, hso);
  STAMP(9);
  {
    float v;
    clock_wait(t4);
    if (w == 7) {
      f32x4 hp[4];
      gather<4, 2>(rw, (lane >> 3) * 4096 + (lane & 7) * 16, WS_HP + j * 128, 32768, T, hp, SPIN_CAP);
      v = rs4((hp[0] + hp[1]) + (hp[2] + hp[3]));
      v += dppf<ROR8>(v);
      v = rbf(xo + rbf(v));
    } else {
      f32x4 dp[2];
      if (gather<2>(rw, vl, dso, 1024, T, dp, SPIN_CAP)) FAIL(8);
      v = rs4(dp[0] + dp[1]);
    }
    *(LAS float*)fw = v;
  }
  barrier();
  STAMP(10);
  if (w == 0) {
    const f32x4 u0 = *(const LAS f32x4*)fr, u1 = *(const LAS f32x4*)(fr + 16);
    float ds = ((u0.x + u0.y) + (u0.z + u0.w)) + ((u1.x + u1.y) + u1.z);
    ds += dppf<ROR8>(ds);
    const float o = u1.w + rbf(ds);
    if ((lane & 8) == 0) __builtin_amdgcn_raw_buffer_store_b16((unsigned short)cvtpk(o, o), rout, ovo, j * 64, 0);
    STAMPX(11);
  }
}

__global__ void __launch_bounds__(512, 2) decode_block_kernel(Params p) {
  __shared__ __attribute__((aligned(16))) unsigned char smem[L_END];
  LAS unsigned char* L = (LAS unsigned char*)smem;
  const int tid = threadIdx.x, lane = tid & 63, w = __builtin_amdgcn_readfirstlane(tid >> 6), b = blockIdx.x;
  const rsrc_t rw = mk(p.ws, WS_END);
  const int w4 = w & 3;
  const u64 tstart = rtc();
  const unsigned t0 = (unsigned)tstart;
  int r0 = w >> 2, r1 = r0, r2 = r0, b0 = b, b1 = b, b2 = b;
  PINS(r1); PINS(r2); PINS(b0); PINS(b1); PINS(b2);
  const int isW = (NF - 1 - b) >> 31, gF = (b * 1171) >> 16, j = b - NF;
  const int grp = (gF & ~isW) | ((j >> 3) & isW), prod = ((b - gF * 56) & ~isW) | ((56 + (j & 7)) & isW);
  Attn A;
  A.rk = mk(p.k, 64 << 20); A.rv = mk(p.v, 64 << 20);
  A.base = grp * (16 << 20) + (((prod * 4 * NTF + w4 * (WMAP ? NTF : 1)) & ~isW) | ((56 * 4 * NTF + (prod - 56) * 4 + w4) & isW)) * 8192;
  A.ring = (unsigned)(size_t)L + w4 * 32768;
#pragma unroll
  for (int jj = 0; jj < 4; ++jj) A.vk[jj] = (lane >> 4) * 256 + (((lane & 15) ^ (4 * (lane >> 4) + jj)) << 4);
  A.lag = 0; A.vn = lane * 16; A.L = L; A.lane = lane; A.w = w4;
  A.t0 = t0 + (STAG ? w4 * (A.k.TAU >> 4) : 0);
  if (tid < 8) *(LAS unsigned*)(L + L_FLAG + lane * 4) = 0u;
  if (r0 != 0) {
    issueK1<0>(A, 0); issueK1<1>(A, 0); issueK1<2>(A, 0); issueK1<3>(A, 0);
  } else {
    u64 pc; int len;
    asm volatile("s_getpc_b64 %0\n.Lcode_from:\n\ts_mov_b32 %1, .Lcode_to-.Lcode_from" : "=s"(pc), "=s"(len));
    asm volatile("s_mov_b32 m0, %0\n\ts_nop 0\n\tbuffer_load_dword %1, %2, %3 offen lds"
                 ::"s"((unsigned)(size_t)L + L_SINK), "v"((lane & 3) * 128), "s"(mk((const void*)pc, len)), "s"(((b >> 3) * 4 + w) * 384) : "memory");
  }
  barrier();
  unsigned tag;
  if (r1 == 0) q_part(p, L, rw, b, w4, lane, t0, tag STAMP_PASS);
  if (b0 < NF) {
    if (r2 != 0) ffn_wave(p, A, rw, b, grp, prod, tag STAMP_PASS);
  }
  if (b1 >= NF) {
    if (r2 != 0) {
      unsigned tg;
      attn_wave<1>(A, []() {}, [&](unsigned) {
        tag = next_tag(__builtin_amdgcn_readfirstlane(__builtin_amdgcn_raw_buffer_load_b32(rw, 0, WS_EPOCH, AUX_SC1)));
        q_sum(L, rw, j, w, lane, t0 + A.k.QD1, tag STAMP_PASS);
      }, tg STAMP_PASS);
    } else {
      q_sum(L, rw, j, w, lane, t0 + A.k.QD1, tag STAMP_PASS);
    }
  }
  if (w == 0) {
    helper_wave(L, rw, lane, grp, tag, t0, A.k STAMP_PASS);
    ml_duty(L, rw, lane, WS_ML + grp * 8192 + prod * 128, tag);
  }
  if (b2 >= NF) {
    barrier();
    const Tag T = mktag(tag);
    if (w >= 4) publish_o(prep_o(L, tid - 256, grp, prod), rw, T);
    STAMP(5);
    wo_tail(p, L, rw, j, w, lane, T, A.k STAMP_PASS);
    asm volatile(".Lcode_to:");
  }
}

extern "C" void kernel_launch(void* const* d_in, const int* in_sizes, int n_in, void* d_out, int out_size, void* d_ws, size_t ws_size, hipStream_t stream) {
  Params p;
  p.x = d_in[0]; p.k = d_in[1]; p.v = d_in[2]; p.wq = d_in[3]; p.wo = d_in[4]; p.wg = d_in[6]; p.wu = d_in[7]; p.wd = d_in[8];
  p.out = d_out; p.ws = d_ws;
  decode_block_kernel<<<256, 512, 0, stream>>>(p);
}
```
